```python
import torch

def _softmax(x, axis=-1):
    return torch.softmax(x, dim=axis)

def _gelu(x):
    return torch.nn.functional.gelu(x, approximate='tanh')

def _silu(x):
    return torch.nn.functional.silu(x)

def _sigmoid(x):
    return torch.sigmoid(x)

def _layernorm(x, axis=-1, eps=1e-05):
    m = x.mean(dim=axis, keepdim=True)
    v = x.var(dim=axis, keepdim=True, unbiased=False)
    return (x - m) / torch.sqrt(v + eps)

def _scatter_add(tgt, idx, src, axis=0):
    return tgt.clone().index_add_(axis, idx, src)

def _segment_sum(src, seg, n):
    out = torch.zeros((n,) + src.shape[1:], dtype=src.dtype)
    return out.index_add_(0, seg, src)

def _pad(x, pw):
    flat = tuple((v for pair in reversed(pw) for v in pair))
    return torch.nn.functional.pad(x, flat)
kernel_name = 'adaLN_cond__mha_block__glu_gated_bilinear__prompt'

def setup_inputs():

    _w = lambda a: (lambda t: t.to(torch.bfloat16) if t.is_floating_point() else t)(torch.as_tensor(a))

    def _A_setup():
        B, S, D, H = (1, 2048, 576, 32)
        return {'x': _w((lambda *s: torch.randn(*s, dtype=torch.bfloat16) * 0.05)(B, S, D)), 'wqkv': _w((lambda *s: torch.randn(*s, dtype=torch.bfloat16) * 0.05)(D, 3 * D)), 'wo': _w((lambda *s: torch.randn(*s, dtype=torch.bfloat16) * 0.05)(D, D))}
    _A_in = _A_setup()

    def _B_setup():
        B, S, D, F = (1, 2048, 576, 4608)
        return {'x': _w((lambda *s: torch.randn(*s, dtype=torch.bfloat16) * 0.05)(B, S, D)), 'w_gate': _w((lambda *s: torch.randn(*s, dtype=torch.bfloat16) * 0.8)(D, F) * D ** (-0.5)), 'w_up': _w((lambda *s: torch.randn(*s, dtype=torch.bfloat16) * 0.8)(D, F) * D ** (-0.5)), 'w_down': _w((lambda *s: torch.randn(*s, dtype=torch.bfloat16) * 0.8)(F, D) * F ** (-0.5))}
    _B_in = _B_setup()
    return {'A_x': _A_in['x'], 'A_wqkv': _A_in['wqkv'], 'A_wo': _A_in['wo'], 'B_x': _B_in['x'], 'B_w_gate': _B_in['w_gate'], 'B_w_up': _B_in['w_up'], 'B_w_down': _B_in['w_down'], 'c': _w((lambda *s: torch.randn(*s, dtype=torch.bfloat16) * 0.2)(1, 576) * 8.0), 'w_mod': _w((lambda *s: torch.randn(*s, dtype=torch.bfloat16) * 0.2)(576, 6 * 576) * 576 ** (-0.5) * 8.0)}

def reference(A_x, A_wqkv, A_wo, B_x, B_w_gate, B_w_up, B_w_down, c, w_mod):

    def _A_ref(x, wqkv, wo):
        B, S, D = x.shape
        H = 32
        Dh = D // H
        qkv = (lambda x, *d: x.permute(*d))((x @ wqkv).reshape(B, S, 3, H, Dh), 2, 0, 3, 1, 4)
        q, k, v = (qkv[0], qkv[1], qkv[2])
        scores = torch.einsum('bhqd,bhkd->bhqk', q, k) * Dh ** (-0.5)
        p = _softmax(scores, axis=-1)
        o = torch.einsum('bhqk,bhkd->bhqd', p, v)
        return (lambda x, *d: x.permute(*d))(o, 0, 2, 1, 3).reshape(B, S, D) @ wo

    def _B_ref(x, w_gate, w_up, w_down):
        g = torch.einsum('bsd,df->bsf', x, w_gate)
        u = torch.einsum('bsd,df->bsf', x, w_up)
        h = g * u
        return torch.einsum('bsf,fd->bsd', h, w_down)
    x = A_x
    mod = (_silu(c) @ w_mod).reshape(c.shape[0], 6, 1, -1)
    sa, ca, ga, sb, cb, gb = (mod[:, 0].clone(), mod[:, 1].clone(), mod[:, 2].clone(), mod[:, 3].clone(), mod[:, 4].clone(), mod[:, 5].clone())
    h = _layernorm(x, axis=-1) * (1 + ca) + sa
    x = x + ga * _A_ref(h, A_wqkv, A_wo)
    h = _layernorm(x, axis=-1) * (1 + cb) + sb
    x = x + gb * _B_ref(h, B_w_gate, B_w_up, B_w_down)
    return x
```

```cpp
#include <hip/hip_runtime.h>
#include <hip/hip_bf16.h>
#include <stdint.h>

typedef short bf16x8 __attribute__((ext_vector_type(8)));
typedef short s16x4 __attribute__((ext_vector_type(4)));
typedef float f32x4 __attribute__((ext_vector_type(4)));
typedef float f32x16 __attribute__((ext_vector_type(16)));
typedef unsigned u32x4 __attribute__((ext_vector_type(4)));
typedef unsigned u32x2 __attribute__((ext_vector_type(2)));
typedef unsigned short u16;
typedef __bf16 bf2_t __attribute__((ext_vector_type(2)));
typedef float f2_t __attribute__((ext_vector_type(2)));

#define DI __device__ __forceinline__
#define LDS_AS __attribute__((address_space(3)))

constexpr int S_ = 2048, D_ = 576, H_ = 32, DH_ = 18, F_ = 4608;
constexpr int NQKV = 3 * D_;

DI float bf2f(unsigned v) { return __uint_as_float(v << 16); }
DI float bfhi2f(unsigned v) { return __uint_as_float(v & 0xffff0000u); }
DI unsigned pack2(float lo, float hi) {
  bf2_t r = __builtin_convertvector((f2_t){lo, hi}, bf2_t);
  return __builtin_bit_cast(unsigned, r);
}
DI float rbf(float x) { return bf2f(pack2(x, 0.f) & 0xffffu); }

constexpr size_t WS_MOD = 0;
constexpr size_t WS_ST1 = 16384;
constexpr size_t WS_QM = 32768;
constexpr size_t WS_QT = WS_QM + (size_t)H_ * S_ * 16 * 2;
constexpr size_t WS_KM = WS_QT + (size_t)H_ * S_ * 2 * 2;
constexpr size_t WS_KT = WS_KM + (size_t)H_ * S_ * 16 * 2;
constexpr size_t WS_VT = WS_KT + (size_t)H_ * S_ * 2 * 2;
constexpr size_t WS_O = WS_VT + (size_t)H_ * DH_ * S_ * 2;
constexpr size_t WS_X1 = WS_O + (size_t)S_ * D_ * 2;
constexpr size_t WS_ST2 = WS_X1 + (size_t)S_ * D_ * 2;
constexpr size_t WS_HH = WS_ST2 + (size_t)S_ * 8 * 4;
constexpr size_t WS_PART = WS_HH + (size_t)S_ * F_ * 2;
constexpr size_t WS_END = WS_PART + (size_t)4 * S_ * D_ * 4;

__global__ __launch_bounds__(256) void k_prep(const u16* __restrict__ c, const u16* __restrict__ wmod,
                                              const u16* __restrict__ x, float* __restrict__ mod,
                                              float* __restrict__ st1) {
  const int tid = threadIdx.x;
  if (blockIdx.x < 108) {
    __shared__ float red[64][33];
    const int tx = tid & 3, ty = tid >> 2;
    const int col0 = blockIdx.x * 32 + tx * 8;
    float acc[8];
#pragma unroll
    for (int j = 0; j < 8; ++j) acc[j] = 0.f;
    u32x4 w[9];
#pragma unroll
    for (int i = 0; i < 9; ++i) w[i] = *(const u32x4*)(wmod + (size_t)(ty + 64 * i) * 3456 + col0);
#pragma unroll
    for (int i = 0; i < 9; ++i) {
      const float cv = bf2f(c[ty + 64 * i]);
      const float s = rbf(cv / (1.f + __expf(-cv)));
#pragma unroll
      for (int j = 0; j < 4; ++j) {
        acc[2 * j] += s * bf2f(w[i][j] & 0xffffu);
        acc[2 * j + 1] += s * bfhi2f(w[i][j]);
      }
    }
#pragma unroll
    for (int j = 0; j < 8; ++j) red[ty][tx * 8 + j] = acc[j];
    __syncthreads();
    if (tid < 32) {
      float s = 0.f;
      for (int r = 0; r < 64; ++r) s += red[r][tid];
      mod[blockIdx.x * 32 + tid] = rbf(s);
    }
  } else {
    const int lane = tid & 63, w = tid >> 6;
    const int row = (blockIdx.x - 108) * 32 + w * 8 + (lane >> 3), j = lane & 7;
    const u16* xr = x + (size_t)row * D_;
    float v[72];
#pragma unroll
    for (int i = 0; i < 9; ++i) {
      u32x4 q = *(const u32x4*)(xr + (j + 8 * i) * 8);
#pragma unroll
      for (int e = 0; e < 4; ++e) {
        v[i * 8 + 2 * e] = bf2f(q[e] & 0xffffu);
        v[i * 8 + 2 * e + 1] = bfhi2f(q[e]);
      }
    }
    float s = 0.f;
#pragma unroll
    for (int i = 0; i < 72; ++i) s += v[i];
    s += __shfl_xor(s, 1); s += __shfl_xor(s, 2); s += __shfl_xor(s, 4);
    const float mean = s * (1.f / 576.f);
    float q2 = 0.f;
#pragma unroll
    for (int i = 0; i < 72; ++i) { const float d = v[i] - mean; q2 += d * d; }
    q2 += __shfl_xor(q2, 1); q2 += __shfl_xor(q2, 2); q2 += __shfl_xor(q2, 4);
    if (j == 0) {
      st1[row * 2] = mean;
      st1[row * 2 + 1] = 1.f / sqrtf(q2 * (1.f / 576.f) + 1e-5f);
    }
  }
}

#define WAIT_VM(n) asm volatile("s_waitcnt vmcnt(" #n ")" ::: "memory")

template <int N> DI void wait_vm() { asm volatile("s_waitcnt vmcnt(%0)" ::"n"(N) : "memory"); }

template <int MT, int NB, int NW, int NSTG, bool LNA>
DI void gemm_core(const u16* __restrict__ Ag,
                  int lda,
                  const u16* __restrict__ Bg,
                  int ldb, int nsteps, char* smem, const float* lnw, int kbeg, const float (&mu)[MT],
                  const float (&rs)[MT], f32x4 (&acc)[MT][NB * 9]) {
  constexpr int STG = NB * 9216 + NW * MT * 1024;
  constexpr int PER = 3 + MT;
  const int tid = threadIdx.x, lane = tid & 63;
  const int w = __builtin_amdgcn_readfirstlane(tid >> 6);
  const int wq = w & 3;
  const int wb = (NB == 2) ? (w >> 2) : 0;
  int boff[3];
#pragma unroll
  for (int i = 0; i < 3; ++i) {
    const int ch = 144 * wq + 64 * i + lane;
    const int pos = ch / 18, cc = ch - pos * 18;
    const int r = (pos & 16) | ((pos & 1) << 3) | ((pos >> 1) & 7);
    boff[i] = r * ldb + cc * 8;
  }
  const u16* aptr = Ag + (size_t)(lane & 15) * lda + 8 * (lane >> 4);
  auto issue = [&](int ks) {
    char* sb = smem + (ks % NSTG) * STG;
#pragma unroll
    for (int mt = 0; mt < MT; ++mt)
      __builtin_amdgcn_global_load_lds((const void*)(aptr + (size_t)mt * 16 * lda + ks * 32),
                                       (LDS_AS void*)(sb + NB * 9216 + (w * MT + mt) * 1024), 16, 0, 0);
    const u16* bb = Bg + (size_t)ks * 32 * ldb;
    char* sbb = sb + wb * 9216 + wq * 144 * 16;
    __builtin_amdgcn_global_load_lds((const void*)(bb + boff[0]), (LDS_AS void*)(sbb), 16, 0, 0);
    __builtin_amdgcn_global_load_lds((const void*)(bb + boff[1]), (LDS_AS void*)(sbb + 1024), 16, 0, 0);
    if (lane < 16) __builtin_amdgcn_global_load_lds((const void*)(bb + boff[2]), (LDS_AS void*)(sbb + 2048), 16, 0, 0);
  };
#pragma unroll
  for (int s = 0; s < NSTG - 1; ++s)
    if (s < nsteps) issue(s);
  const int g = lane >> 4, q = (lane & 15) >> 2, p = lane & 3;
  const int brd = (16 * (g >> 1) + 2 * q + (g & 1)) * 288 + 8 * p;
  for (int ks = 0; ks < nsteps; ++ks) {
    if (ks + NSTG - 2 < nsteps) wait_vm<(NSTG - 2) * PER>();
    else wait_vm<0>();
    __builtin_amdgcn_s_barrier();
    asm volatile("" ::: "memory");
    if (ks + NSTG - 1 < nsteps) issue(ks + NSTG - 1);
    char* sb = smem + (ks % NSTG) * STG;
    bf16x8 af[MT];
#pragma unroll
    for (int mt = 0; mt < MT; ++mt) {
      u32x4 raw = *(const u32x4*)(sb + NB * 9216 + (w * MT + mt) * 1024 + lane * 16);
      if constexpr (LNA) {
        const int k = kbeg + ks * 32 + 8 * g;
        const f32x4 w0 = *(const f32x4*)(lnw + k), w1 = *(const f32x4*)(lnw + k + 4);
        const f32x4 b0 = *(const f32x4*)(lnw + 576 + k), b1 = *(const f32x4*)(lnw + 576 + k + 4);
        const float nm = -mu[mt] * rs[mt];
        u32x4 o;
#pragma unroll
        for (int e = 0; e < 4; ++e) {
          const float lo = fmaf(bf2f(raw[e] & 0xffffu), rs[mt], nm);
          const float hi = fmaf(bfhi2f(raw[e]), rs[mt], nm);
          const float wl = e < 2 ? w0[2 * e] : w1[2 * e - 4], wh = e < 2 ? w0[2 * e + 1] : w1[2 * e - 3];
          const float bl = e < 2 ? b0[2 * e] : b1[2 * e - 4], bh = e < 2 ? b0[2 * e + 1] : b1[2 * e - 3];
          o[e] = pack2(fmaf(lo, wl, bl), fmaf(hi, wh, bh));
        }
        raw = o;
      }
      af[mt] = __builtin_bit_cast(bf16x8, raw);
    }
#pragma unroll
    for (int b = 0; b < NB; ++b) {
#pragma unroll
      for (int t = 0; t < 9; ++t) {
        const char* bp = sb + b * 9216 + brd + t * 32;
        s16x4 lo = __builtin_amdgcn_ds_read_tr16_b64_v4i16((LDS_AS s16x4*)(bp));
        s16x4 hi = __builtin_amdgcn_ds_read_tr16_b64_v4i16((LDS_AS s16x4*)(bp + 2304));
        bf16x8 bf = __builtin_shufflevector(lo, hi, 0, 1, 2, 3, 4, 5, 6, 7);
#pragma unroll
        for (int mt = 0; mt < MT; ++mt)
          acc[mt][b * 9 + t] = __builtin_amdgcn_mfma_f32_16x16x32_bf16(bf, af[mt], acc[mt][b * 9 + t], 0, 0, 0);
      }
    }
  }
}

constexpr int K2_NSTG = 4;
constexpr int K2_STG = 9216 + 4 * 2 * 1024;
constexpr int K2_SMEM = K2_NSTG * K2_STG + 2 * 576 * 4;
__global__ __launch_bounds__(256) void k_qkv(const u16* __restrict__ x, const u16* __restrict__ wqkv,
                                             const float* __restrict__ mod, const float* __restrict__ st1,
                                             u16* __restrict__ Qm, u16* __restrict__ Qt, u16* __restrict__ Km,
                                             u16* __restrict__ Kt, u16* __restrict__ Vt) {
  extern __shared__ __attribute__((aligned(16))) char smem[];
  const int tid = threadIdx.x, lane = tid & 63, w = tid >> 6;
  const int bm = blockIdx.x & 15, bn = blockIdx.x >> 4;
  float* lnw = (float*)(smem + K2_NSTG * K2_STG);
  for (int i = tid; i < 576; i += 256) {
    lnw[i] = 1.f + mod[576 + i];
    lnw[576 + i] = mod[i];
  }
  const int row0 = bm * 128 + w * 32;
  float mu[2], rs[2];
#pragma unroll
  for (int mt = 0; mt < 2; ++mt) {
    const int r = row0 + mt * 16 + (lane & 15);
    mu[mt] = st1[r * 2];
    rs[mt] = st1[r * 2 + 1];
  }
  __syncthreads();
  f32x4 acc[2][9];
#pragma unroll
  for (int i = 0; i < 2; ++i)
#pragma unroll
    for (int j = 0; j < 9; ++j) acc[i][j] = (f32x4){0.f, 0.f, 0.f, 0.f};
  gemm_core<2, 1, 4, K2_NSTG, true>(x + (size_t)row0 * D_, D_, wqkv + bn * 144, NQKV, 18, smem, lnw, 0, mu, rs, acc);
  const float qscale = 0.23570226039551584f * 1.4426950408889634f;
  const int which = bn >> 2;
#pragma unroll
  for (int mt = 0; mt < 2; ++mt) {
    const int r = row0 + mt * 16 + (lane & 15);
#pragma unroll
    for (int t = 0; t < 9; ++t) {
#pragma unroll
      for (int j = 0; j < 4; ++j) {
        const int n = (bn & 3) * 144 + t * 16 + 4 * (lane >> 4) + j;
        const int h = n / 18, d = n - h * 18;
        float v = acc[mt][t][j];
        if (which == 0) {
          const u16 b = (u16)(pack2(v * qscale, 0.f) & 0xffffu);
          if (d < 16) Qm[((size_t)h * S_ + r) * 16 + d] = b;
          else Qt[((size_t)h * S_ + r) * 2 + (d - 16)] = b;
        } else if (which == 1) {
          const u16 b = (u16)(pack2(v, 0.f) & 0xffffu);
          if (d < 16) Km[((size_t)h * S_ + r) * 16 + d] = b;
          else Kt[((size_t)h * S_ + r) * 2 + (d - 16)] = b;
        } else {
          const u16 b = (u16)(pack2(v, 0.f) & 0xffffu);
          const int kk = r & 31;
          const int pos = (kk & 16) | ((kk & 4) << 1) | ((kk & 8) >> 1) | (kk & 3);
          Vt[((size_t)h * DH_ + d) * S_ + (r & ~31) + pos] = b;
        }
      }
    }
  }
}

constexpr int VROW = 4096 + 16;
constexpr int A_KM = 0, A_KT = 65536, A_VT = 65536 + 8192, A_RED = A_VT + 20 * VROW;
constexpr int A_SMEM = A_RED + 64;
__global__ __launch_bounds__(512) void k_attn(const u16* __restrict__ Qm, const u16* __restrict__ Qt,
                                              const u16* __restrict__ Km, const u16* __restrict__ Kt,
                                              const u16* __restrict__ Vt, u16* __restrict__ o) {
  extern __shared__ __attribute__((aligned(16))) char smem[];
  const int tid = threadIdx.x, lane = tid & 63;
  const int w = __builtin_amdgcn_readfirstlane(tid >> 6);
  const int bid = blockIdx.x;
  const int h = (bid & 7) * 4 + ((bid >> 3) & 3), qc = bid >> 5;
  const u16* kmg = Km + (size_t)h * S_ * 16;
  const u16* ktg = Kt + (size_t)h * S_ * 2;
  const u16* vtg = Vt + (size_t)h * DH_ * S_;
#pragma unroll
  for (int i = 0; i < 8; ++i)
    __builtin_amdgcn_global_load_lds((const void*)(kmg + (w * 8 + i) * 512 + lane * 8),
                                     (LDS_AS void*)(smem + A_KM + (w * 8 + i) * 1024), 16, 0, 0);
  __builtin_amdgcn_global_load_lds((const void*)(ktg + w * 512 + lane * 8), (LDS_AS void*)(smem + A_KT + w * 1024), 16, 0, 0);
#pragma unroll
  for (int i = 0; i < 9; ++i) {
    const int idx = w * 9 + i, d = idx >> 2, part = idx & 3;
    __builtin_amdgcn_global_load_lds((const void*)(vtg + d * S_ + part * 512 + lane * 8),
                                     (LDS_AS void*)(smem + A_VT + d * VROW + part * 1024), 16, 0, 0);
  }
  for (int i = tid; i < 512; i += 512) {
    const int rr = 18 + (i >> 8), cidx = i & 255;
    const unsigned val = rr == 18 ? 0x3f803f80u : 0u;
    *(u32x4*)(smem + A_VT + rr * VROW + cidx * 16) = (u32x4){val, val, val, val};
  }
  const int r32 = lane & 31, hh = lane >> 5;
  const int qrow = qc * 256 + w * 32 + r32;
  bf16x8 qf0 = *(const bf16x8*)(Qm + ((size_t)h * S_ + qrow) * 16 + hh * 8);
  unsigned qtw = hh == 0 ? *(const unsigned*)(Qt + ((size_t)h * S_ + qrow) * 2) : 0u;
  bf16x8 qf1 = __builtin_bit_cast(bf16x8, (u32x4){qtw, 0u, 0u, 0u});
  float qn = 0.f;
  {
    u32x4 qq = __builtin_bit_cast(u32x4, qf0);
#pragma unroll
    for (int e = 0; e < 4; ++e) {
      const float a = bf2f(qq[e] & 0xffffu), b = bfhi2f(qq[e]);
      qn += a * a + b * b;
    }
    const float a = bf2f(qtw & 0xffffu), b = bfhi2f(qtw);
    qn += a * a + b * b;
    qn += __shfl_xor(qn, 32);
  }
  asm volatile("s_waitcnt vmcnt(0)" ::: "memory");
  __syncthreads();
  {
    float kmax = 0.f;
#pragma unroll
    for (int i = 0; i < 4; ++i) {
      const int key = tid + 512 * i;
      u32x4 a = *(const u32x4*)(smem + A_KM + key * 32), b = *(const u32x4*)(smem + A_KM + key * 32 + 16);
      const unsigned t = *(const unsigned*)(smem + A_KT + key * 4);
      float s = 0.f;
#pragma unroll
      for (int e = 0; e < 4; ++e) {
        float u0 = bf2f(a[e] & 0xffffu), u1 = bfhi2f(a[e]), u2 = bf2f(b[e] & 0xffffu), u3 = bfhi2f(b[e]);
        s += u0 * u0 + u1 * u1 + u2 * u2 + u3 * u3;
      }
      float u0 = bf2f(t & 0xffffu), u1 = bfhi2f(t);
      s += u0 * u0 + u1 * u1;
      kmax = fmaxf(kmax, s);
    }
#pragma unroll
    for (int off = 1; off < 64; off <<= 1) kmax = fmaxf(kmax, __shfl_xor(kmax, off));
    if (lane == 0) *(float*)(smem + A_RED + w * 4) = kmax;
  }
  __syncthreads();
  float kn = 0.f;
#pragma unroll
  for (int i = 0; i < 8; ++i) kn = fmaxf(kn, *(const float*)(smem + A_RED + i * 4));
  float m = sqrtf(qn * kn) * 1.0001f + 1e-6f;

  const char* kmp = smem + A_KM + r32 * 32 + hh * 16;
  const char* ktp = hh == 0 ? (smem + A_KT + r32 * 4) : (smem + A_VT + 19 * VROW);
  const int ktstep = hh == 0 ? 128 : 0;
  const int vrow = r32 < 19 ? r32 : 19;
  const char* vp = smem + A_VT + vrow * VROW + hh * 16;

  f32x16 oacc;
  for (int attempt = 0; attempt < 2; ++attempt) {
    f32x16 negm;
#pragma unroll
    for (int i = 0; i < 16; ++i) { negm[i] = -m; oacc[i] = 0.f; }
    auto qk = [&](int t) -> f32x16 {
      bf16x8 kf0 = *(const bf16x8*)(kmp + t * 1024);
      unsigned kt = *(const unsigned*)(ktp + t * ktstep);
      bf16x8 kf1 = __builtin_bit_cast(bf16x8, (u32x4){kt, 0u, 0u, 0u});
      f32x16 s = __builtin_amdgcn_mfma_f32_32x32x16_bf16(kf0, qf0, negm, 0, 0, 0);
      return __builtin_amdgcn_mfma_f32_32x32x16_bf16(kf1, qf1, s, 0, 0, 0);
    };
    auto pv = [&](int t, const f32x16& s) {
      u32x4 p0, p1;
#pragma unroll
      for (int e = 0; e < 4; ++e) {
        p0[e] = pack2(__builtin_amdgcn_exp2f(s[2 * e]), __builtin_amdgcn_exp2f(s[2 * e + 1]));
        p1[e] = pack2(__builtin_amdgcn_exp2f(s[8 + 2 * e]), __builtin_amdgcn_exp2f(s[8 + 2 * e + 1]));
      }
      bf16x8 vf0 = *(const bf16x8*)(vp + t * 64);
      bf16x8 vf1 = *(const bf16x8*)(vp + t * 64 + 32);
      oacc = __builtin_amdgcn_mfma_f32_32x32x16_bf16(vf0, __builtin_bit_cast(bf16x8, p0), oacc, 0, 0, 0);
      oacc = __builtin_amdgcn_mfma_f32_32x32x16_bf16(vf1, __builtin_bit_cast(bf16x8, p1), oacc, 0, 0, 0);
    };
    f32x16 sA = qk(0), sB;
    for (int t = 0; t < 64; t += 2) {
      sB = qk(t + 1);
      pv(t, sA);
      if (t + 2 < 64) sA = qk(t + 2);
      pv(t + 1, sB);
    }
    const float l = __shfl(oacc[10], r32);
    if (attempt == 1 || __all(l >= 8.271806e-25f  )) break;
    float tm = -3.0e38f;
    f32x16 zero;
#pragma unroll
    for (int i = 0; i < 16; ++i) zero[i] = 0.f;
    for (int t = 0; t < 64; ++t) {
      bf16x8 kf0 = *(const bf16x8*)(kmp + t * 1024);
      unsigned kt = *(const unsigned*)(ktp + t * ktstep);
      bf16x8 kf1 = __builtin_bit_cast(bf16x8, (u32x4){kt, 0u, 0u, 0u});
      f32x16 s = __builtin_amdgcn_mfma_f32_32x32x16_bf16(kf0, qf0, zero, 0, 0, 0);
      s = __builtin_amdgcn_mfma_f32_32x32x16_bf16(kf1, qf1, s, 0, 0, 0);
#pragma unroll
      for (int i = 0; i < 16; ++i) tm = fmaxf(tm, s[i]);
    }
    tm = fmaxf(tm, __shfl_xor(tm, 32));
    m = tm;
  }
  const float l = __shfl(oacc[10], r32);
  const float inv = 1.f / l;
  u16* op = o + (size_t)qrow * D_ + h * DH_;
  {
    u32x2 a = {pack2(oacc[0] * inv, oacc[1] * inv), pack2(oacc[2] * inv, oacc[3] * inv)};
    u32x2 b = {pack2(oacc[4] * inv, oacc[5] * inv), pack2(oacc[6] * inv, oacc[7] * inv)};
    unsigned* p32 = (unsigned*)(op + hh * 4);
    p32[0] = a[0]; p32[1] = a[1];
    p32[4] = b[0]; p32[5] = b[1];
    if (hh == 0) *(unsigned*)(op + 16) = pack2(oacc[8] * inv, oacc[9] * inv);
  }
}

constexpr int K4_NSTG = 4;
constexpr int K4_STG = 9216 + 4 * 1024;
constexpr int K4_SMEM = K4_NSTG * K4_STG;
__global__ __launch_bounds__(256) void k_wo(const u16* __restrict__ o, const u16* __restrict__ wo,
                                            const u16* __restrict__ x, const float* __restrict__ mod,
                                            u16* __restrict__ x1, float* __restrict__ st2) {
  extern __shared__ __attribute__((aligned(16))) char smem[];
  const int tid = threadIdx.x, lane = tid & 63, w = tid >> 6;
  const int bm = blockIdx.x & 31, bn = blockIdx.x >> 5;
  const int row0 = bm * 64 + w * 16;
  float mu[1] = {0.f}, rs[1] = {0.f};
  f32x4 acc[1][9];
#pragma unroll
  for (int j = 0; j < 9; ++j) acc[0][j] = (f32x4){0.f, 0.f, 0.f, 0.f};
  gemm_core<1, 1, 4, K4_NSTG, false>(o + (size_t)row0 * D_, D_, wo + bn * 144, D_, 18, smem, nullptr, 0, mu, rs, acc);
  const int r = row0 + (lane & 15);
  const float piv = bf2f(x[(size_t)r * D_]);
  float s1 = 0.f, s2 = 0.f;
#pragma unroll
  for (int t = 0; t < 9; ++t) {
    const int n = bn * 144 + t * 16 + 4 * (lane >> 4);
    const u32x2 xv = *(const u32x2*)(x + (size_t)r * D_ + n);
    const f32x4 gv = *(const f32x4*)(mod + 2 * 576 + n);
    float y[4];
    y[0] = rbf(bf2f(xv[0] & 0xffffu) + rbf(gv[0] * rbf(acc[0][t][0])));
    y[1] = rbf(bfhi2f(xv[0]) + rbf(gv[1] * rbf(acc[0][t][1])));
    y[2] = rbf(bf2f(xv[1] & 0xffffu) + rbf(gv[2] * rbf(acc[0][t][2])));
    y[3] = rbf(bfhi2f(xv[1]) + rbf(gv[3] * rbf(acc[0][t][3])));
#pragma unroll
    for (int j = 0; j < 4; ++j) { const float dd = y[j] - piv; s1 += dd; s2 += dd * dd; }
    *(u32x2*)(x1 + (size_t)r * D_ + n) = (u32x2){pack2(y[0], y[1]), pack2(y[2], y[3])};
  }
  s1 += __shfl_xor(s1, 16); s1 += __shfl_xor(s1, 32);
  s2 += __shfl_xor(s2, 16); s2 += __shfl_xor(s2, 32);
  if (lane < 16) {
    st2[(size_t)r * 8 + bn * 2] = s1;
    st2[(size_t)r * 8 + bn * 2 + 1] = s2;
  }
}

constexpr int K5_NSTG = 4;
constexpr int K5_STG = 2 * 9216 + 8 * 2 * 1024;
constexpr int K5_SMEM = K5_NSTG * K5_STG + 2 * 576 * 4;
__global__ __launch_bounds__(512) void k_gateup(const u16* __restrict__ x1, const u16* __restrict__ xin,
                                                const u16* __restrict__ wg, const u16* __restrict__ wu,
                                                const float* __restrict__ mod, const float* __restrict__ st2,
                                                u16* __restrict__ hh) {
  extern __shared__ __attribute__((aligned(16))) char smem[];
  const int tid = threadIdx.x, lane = tid & 63, w = tid >> 6;
  const int bm = blockIdx.x & 7, bn = blockIdx.x >> 3;
  float* lnw = (float*)(smem + K5_NSTG * K5_STG);
  for (int i = tid; i < 576; i += 512) {
    lnw[i] = 1.f + mod[4 * 576 + i];
    lnw[576 + i] = mod[3 * 576 + i];
  }
  const int row0 = bm * 256 + w * 32;
  float mu[2], rs[2];
#pragma unroll
  for (int mt = 0; mt < 2; ++mt) {
    const int r = row0 + mt * 16 + (lane & 15);
    const f32x4 a = *(const f32x4*)(st2 + (size_t)r * 8), b = *(const f32x4*)(st2 + (size_t)r * 8 + 4);
    const float s1 = (a[0] + a[2]) + (b[0] + b[2]), s2 = (a[1] + a[3]) + (b[1] + b[3]);
    const float piv = bf2f(xin[(size_t)r * D_]);
    const float dm = s1 * (1.f / 576.f);
    const float var = fmaxf(s2 * (1.f / 576.f) - dm * dm, 0.f);
    mu[mt] = piv + dm;
    rs[mt] = 1.f / sqrtf(var + 1e-5f);
  }
  __syncthreads();
  f32x4 acc[2][18];
#pragma unroll
  for (int i = 0; i < 2; ++i)
#pragma unroll
    for (int j = 0; j < 18; ++j) acc[i][j] = (f32x4){0.f, 0.f, 0.f, 0.f};
  const u16* bsel = (w < 4 ? wg : wu) + bn * 144;
  gemm_core<2, 2, 8, K5_NSTG, true>(x1 + (size_t)row0 * D_, D_, bsel, F_, 18, smem, lnw, 0, mu, rs, acc);
#pragma unroll
  for (int mt = 0; mt < 2; ++mt) {
    const int r = row0 + mt * 16 + (lane & 15);
#pragma unroll
    for (int t = 0; t < 9; ++t) {
      const int n = bn * 144 + t * 16 + 4 * (lane >> 4);
      float y[4];
#pragma unroll
      for (int j = 0; j < 4; ++j) y[j] = rbf(acc[mt][t][j]) * rbf(acc[mt][9 + t][j]);
      *(u32x2*)(hh + (size_t)r * F_ + n) = (u32x2){pack2(y[0], y[1]), pack2(y[2], y[3])};
    }
  }
}

constexpr int K6_NSTG = 6;
constexpr int K6_STG = 9216 + 4 * 2 * 1024;
constexpr int K6_SMEM = K6_NSTG * K6_STG;
__global__ __launch_bounds__(256) void k_down(const u16* __restrict__ hh, const u16* __restrict__ wd,
                                              float* __restrict__ part) {
  extern __shared__ __attribute__((aligned(16))) char smem[];
  const int tid = threadIdx.x, lane = tid & 63, w = tid >> 6;
  const int bm = blockIdx.x & 15, bn = (blockIdx.x >> 4) & 3, sk = blockIdx.x >> 6;
  const int row0 = bm * 128 + w * 32;
  float mu[2] = {0.f, 0.f}, rs[2] = {0.f, 0.f};
  f32x4 acc[2][9];
#pragma unroll
  for (int i = 0; i < 2; ++i)
#pragma unroll
    for (int j = 0; j < 9; ++j) acc[i][j] = (f32x4){0.f, 0.f, 0.f, 0.f};
  gemm_core<2, 1, 4, K6_NSTG, false>(hh + (size_t)row0 * F_ + sk * 1152, F_, wd + (size_t)sk * 1152 * D_ + bn * 144, D_, 36,
                                     smem, nullptr, 0, mu, rs, acc);
  float* pp = part + (size_t)sk * S_ * D_;
#pragma unroll
  for (int mt = 0; mt < 2; ++mt) {
    const int r = row0 + mt * 16 + (lane & 15);
#pragma unroll
    for (int t = 0; t < 9; ++t) {
      const int n = bn * 144 + t * 16 + 4 * (lane >> 4);
      *(f32x4*)(pp + (size_t)r * D_ + n) = acc[mt][t];
    }
  }
}

__global__ __launch_bounds__(256) void k_final(const float* __restrict__ part, const u16* __restrict__ x1,
                                               const float* __restrict__ mod, u16* __restrict__ out) {
  const int i = blockIdx.x * 256 + threadIdx.x;
  const int e = i * 8;
  const int n = e % D_;
  f32x4 a0 = *(const f32x4*)(part + e), a1 = *(const f32x4*)(part + e + 4);
#pragma unroll
  for (int s = 1; s < 4; ++s) {
    a0 += *(const f32x4*)(part + (size_t)s * S_ * D_ + e);
    a1 += *(const f32x4*)(part + (size_t)s * S_ * D_ + e + 4);
  }
  const u32x4 xv = *(const u32x4*)(x1 + e);
  const f32x4 g0 = *(const f32x4*)(mod + 5 * 576 + n), g1 = *(const f32x4*)(mod + 5 * 576 + n + 4);
  u32x4 r;
  r[0] = pack2(bf2f(xv[0] & 0xffffu) + rbf(g0[0] * rbf(a0[0])), bfhi2f(xv[0]) + rbf(g0[1] * rbf(a0[1])));
  r[1] = pack2(bf2f(xv[1] & 0xffffu) + rbf(g0[2] * rbf(a0[2])), bfhi2f(xv[1]) + rbf(g0[3] * rbf(a0[3])));
  r[2] = pack2(bf2f(xv[2] & 0xffffu) + rbf(g1[0] * rbf(a1[0])), bfhi2f(xv[2]) + rbf(g1[1] * rbf(a1[1])));
  r[3] = pack2(bf2f(xv[3] & 0xffffu) + rbf(g1[2] * rbf(a1[2])), bfhi2f(xv[3]) + rbf(g1[3] * rbf(a1[3])));
  *(u32x4*)(out + e) = r;
}

extern "C" void kernel_launch(void* const* d_in, const int* in_sizes, int n_in, void* d_out, int out_size,
                              void* d_ws, size_t ws_size, hipStream_t stream) {
  const u16* x = (const u16*)d_in[0];
  const u16* wqkv = (const u16*)d_in[1];
  const u16* wo = (const u16*)d_in[2];
  const u16* wg = (const u16*)d_in[4];
  const u16* wu = (const u16*)d_in[5];
  const u16* wd = (const u16*)d_in[6];
  const u16* c = (const u16*)d_in[7];
  const u16* wmod = (const u16*)d_in[8];
  char* ws = (char*)d_ws;
  float* mod = (float*)(ws + WS_MOD);
  float* st1 = (float*)(ws + WS_ST1);
  u16* Qm = (u16*)(ws + WS_QM);
  u16* Qt = (u16*)(ws + WS_QT);
  u16* Km = (u16*)(ws + WS_KM);
  u16* Kt = (u16*)(ws + WS_KT);
  u16* Vt = (u16*)(ws + WS_VT);
  u16* o = (u16*)(ws + WS_O);
  u16* x1 = (u16*)(ws + WS_X1);
  float* st2 = (float*)(ws + WS_ST2);
  u16* hh = (u16*)(ws + WS_HH);
  float* part = (float*)(ws + WS_PART);

  (void)hipFuncSetAttribute((const void*)k_qkv, hipFuncAttributeMaxDynamicSharedMemorySize, K2_SMEM);
  (void)hipFuncSetAttribute((const void*)k_attn, hipFuncAttributeMaxDynamicSharedMemorySize, A_SMEM);
  (void)hipFuncSetAttribute((const void*)k_wo, hipFuncAttributeMaxDynamicSharedMemorySize, K4_SMEM);
  (void)hipFuncSetAttribute((const void*)k_gateup, hipFuncAttributeMaxDynamicSharedMemorySize, K5_SMEM);
  (void)hipFuncSetAttribute((const void*)k_down, hipFuncAttributeMaxDynamicSharedMemorySize, K6_SMEM);

  k_prep<<<108 + 64, 256, 0, stream>>>(c, wmod, x, mod, st1);
  k_qkv<<<16 * 12, 256, K2_SMEM, stream>>>(x, wqkv, mod, st1, Qm, Qt, Km, Kt, Vt);
  k_attn<<<256, 512, A_SMEM, stream>>>(Qm, Qt, Km, Kt, Vt, o);
  k_wo<<<32 * 4, 256, K4_SMEM, stream>>>(o, wo, x, mod, x1, st2);
  k_gateup<<<8 * 32, 512, K5_SMEM, stream>>>(x1, x, wg, wu, mod, st2, hh);
  k_down<<<16 * 4 * 4, 256, K6_SMEM, stream>>>(hh, wd, part);
  k_final<<<S_ * D_ / 8 / 256, 256, 0, stream>>>(part, x1, mod, (u16*)d_out);
}
```

```cpp
#include <hip/hip_runtime.h>
#include <hip/hip_bf16.h>
#include <stdint.h>

typedef short bf16x8 __attribute__((ext_vector_type(8)));
typedef short s16x4 __attribute__((ext_vector_type(4)));
typedef float f32x4 __attribute__((ext_vector_type(4)));
typedef float f32x16 __attribute__((ext_vector_type(16)));
typedef unsigned u32x4 __attribute__((ext_vector_type(4)));
typedef unsigned u32x2 __attribute__((ext_vector_type(2)));
typedef unsigned short u16;
typedef __bf16 bf2_t __attribute__((ext_vector_type(2)));
typedef float f2_t __attribute__((ext_vector_type(2)));

#define DI __device__ __forceinline__
#define ST_OUT(p, v) (*(p) = (v))
#define LDS_AS __attribute__((address_space(3)))

constexpr int S_ = 2048, D_ = 576, H_ = 32, DH_ = 18, F_ = 4608;
constexpr int NQKV = 3 * D_;

DI float bf2f(unsigned v) { return __uint_as_float(v << 16); }
DI float bfhi2f(unsigned v) { return __uint_as_float(v & 0xffff0000u); }
DI unsigned pack2(float lo, float hi) {
  bf2_t r = __builtin_convertvector((f2_t){lo, hi}, bf2_t);
  return __builtin_bit_cast(unsigned, r);
}
DI float rbf(float x) { return bf2f(pack2(x, 0.f) & 0xffffu); }

constexpr size_t WS_MOD = 0;
constexpr size_t WS_ST1 = 16384;
constexpr size_t WS_QM = 32768;
constexpr size_t WS_QT = WS_QM + (size_t)H_ * S_ * 16 * 2;
constexpr size_t WS_KM = WS_QT + (size_t)H_ * S_ * 2 * 2;
constexpr size_t WS_KT = WS_KM + (size_t)H_ * S_ * 16 * 2;
constexpr size_t WS_VT = WS_KT + (size_t)H_ * S_ * 2 * 2;
constexpr size_t WS_O = WS_VT + (size_t)H_ * DH_ * S_ * 2;
constexpr size_t WS_X1 = WS_O + (size_t)S_ * D_ * 2;
constexpr size_t WS_ST2 = WS_X1 + (size_t)S_ * D_ * 2;
constexpr size_t WS_HH = WS_ST2 + (size_t)S_ * 8 * 4;
constexpr size_t WS_PART = WS_HH + (size_t)S_ * F_ * 2;
constexpr size_t WS_END = WS_PART + (size_t)4 * S_ * D_ * 4;

__global__ __launch_bounds__(256) void k_prep(const u16* __restrict__ c, const u16* __restrict__ wmod,
                                              const u16* __restrict__ x, float* __restrict__ mod,
                                              float* __restrict__ st1) {
  const int tid = threadIdx.x;
  if (blockIdx.x < 108) {
    __shared__ float red[64][33];
    const int tx = tid & 3, ty = tid >> 2;
    const int col0 = blockIdx.x * 32 + tx * 8;
    float acc[8];
#pragma unroll
    for (int j = 0; j < 8; ++j) acc[j] = 0.f;
    u32x4 w[9];
#pragma unroll
    for (int i = 0; i < 9; ++i) w[i] = *(const u32x4*)(wmod + (size_t)(ty + 64 * i) * 3456 + col0);
#pragma unroll
    for (int i = 0; i < 9; ++i) {
      const float cv = bf2f(c[ty + 64 * i]);
      const float s = rbf(cv / (1.f + __expf(-cv)));
#pragma unroll
      for (int j = 0; j < 4; ++j) {
        acc[2 * j] += s * bf2f(w[i][j] & 0xffffu);
        acc[2 * j + 1] += s * bfhi2f(w[i][j]);
      }
    }
#pragma unroll
    for (int j = 0; j < 8; ++j) red[ty][tx * 8 + j] = acc[j];
    __syncthreads();
    if (tid < 32) {
      float s = 0.f;
      for (int r = 0; r < 64; ++r) s += red[r][tid];
      mod[blockIdx.x * 32 + tid] = rbf(s);
    }
  } else {
    const int lane = tid & 63, w = tid >> 6;
    const int row = (blockIdx.x - 108) * 32 + w * 8 + (lane >> 3), j = lane & 7;
    const u16* xr = x + (size_t)row * D_;
    float v[72];
#pragma unroll
    for (int i = 0; i < 9; ++i) {
      u32x4 q = *(const u32x4*)(xr + (j + 8 * i) * 8);
#pragma unroll
      for (int e = 0; e < 4; ++e) {
        v[i * 8 + 2 * e] = bf2f(q[e] & 0xffffu);
        v[i * 8 + 2 * e + 1] = bfhi2f(q[e]);
      }
    }
    float s = 0.f;
#pragma unroll
    for (int i = 0; i < 72; ++i) s += v[i];
    s += __shfl_xor(s, 1); s += __shfl_xor(s, 2); s += __shfl_xor(s, 4);
    const float mean = s * (1.f / 576.f);
    float q2 = 0.f;
#pragma unroll
    for (int i = 0; i < 72; ++i) { const float d = v[i] - mean; q2 += d * d; }
    q2 += __shfl_xor(q2, 1); q2 += __shfl_xor(q2, 2); q2 += __shfl_xor(q2, 4);
    if (j == 0) {
      st1[row * 2] = mean;
      st1[row * 2 + 1] = 1.f / sqrtf(q2 * (1.f / 576.f) + 1e-5f);
    }
  }
}

#define WAIT_VM(n) asm volatile("s_waitcnt vmcnt(" #n ")" ::: "memory")

template <int N> DI void wait_vm() { asm volatile("s_waitcnt vmcnt(%0)" ::"n"(N) : "memory"); }

#define ISSUE_DIV 2
template <int BM, int NB, int NKS, int NW, int NSTG, bool LNA, bool SWAP, int MT = 4, bool AFRAG = false>
DI void gemm_core2(const u16* __restrict__ Ag,
                   int lda, const u16* __restrict__ B0, const u16* __restrict__ B1,
                   int ldb, int nstages, char* smem, const float* lnw, int kbeg, const float (&mu)[MT],
                   const float (&rs)[MT], f32x4 (&acc)[MT][9]) {
  constexpr int RG = BM / (16 * MT);
  constexpr int NWORK = NB * NKS * RG / NW;
  constexpr int BBYTES = NB * NKS * 9216;
  constexpr int STG = BBYTES + (BM / 16) * NKS * 1024;
  constexpr int APW = (BM / 16) * NKS / NW;
  constexpr int BPW = NB * NKS * 576 / NW;
  constexpr int NBI = (BPW + 63) / 64;
  constexpr int BREM = BPW - (NBI - 1) * 64;
  constexpr int PER = APW + NBI;
  const int tid = threadIdx.x, lane = tid & 63;
  const int w = __builtin_amdgcn_readfirstlane(tid >> 6);
  const int rg = w % RG, wj = w / RG;
  const u16* Bw = ((BPW * w) / (576 * NKS) == 0) ? B0 : B1;
  unsigned bsrc[NBI];
#pragma unroll
  for (int i = 0; i < NBI; ++i) {
    const int c = BPW * w + 64 * i + (lane < BREM || i < NBI - 1 ? lane : 0);
    const int st = c / 576, cin = c - st * 576;
    const int ks = st % NKS;
    const int pos = cin / 18, cc = cin - pos * 18;
    const int r = (pos & 16) | ((pos & 1) << 3) | ((pos >> 1) & 7);
    bsrc[i] = (unsigned)((ks * 32 + r) * ldb + cc * 8);
  }
  unsigned asrc[APW];
#pragma unroll
  for (int i = 0; i < APW; ++i) {
    if constexpr (AFRAG) {
      const int blk = w * APW + i, ks = blk / (BM / 16), rt = blk - ks * (BM / 16);
      asrc[i] = (unsigned)(((rt * lda + ks) * 64 + lane) * 8);
    } else {
      const int blk = w * APW + i, kp = blk / (BM / 8), r8 = blk - kp * (BM / 8);
      asrc[i] = (unsigned)((r8 * 8 + (lane >> 3)) * lda + kp * 64 + (((lane & 7) ^ (lane >> 3)) * 8));
    }
  }
  auto issue_one = [&](int s, int i) {
    char* sb = smem + (s % NSTG) * STG;
    if (i < APW) {
      __builtin_amdgcn_global_load_lds((const void*)(Ag + s * NKS * (AFRAG ? 512 : 32) + asrc[i]), (LDS_AS void*)(sb + BBYTES + (w * APW + i) * 1024), 16, 0, 0);
    } else {
      const int j = i - APW;
      const u16* bb = Bw + (size_t)s * NKS * 32 * ldb;
      if (j < NBI - 1 || BREM == 64 || lane < BREM)
        __builtin_amdgcn_global_load_lds((const void*)(bb + bsrc[j]), (LDS_AS void*)(sb + (BPW * w + 64 * j) * 16), 16, 0, 0);
    }
  };
#pragma unroll
  for (int s = 0; s < NSTG - 1; ++s)
    if (s < nstages) {
#pragma unroll
      for (int i = 0; i < PER; ++i) issue_one(s, i);
    }
  const int g = lane >> 4, q = (lane & 15) >> 2, p = lane & 3;
  const int brd = (16 * (g >> 1) + 2 * q + (g & 1)) * 288 + 8 * p;
  for (int s = 0; s < nstages; ++s) {
    if (s + NSTG - 2 < nstages) wait_vm<(NSTG - 2) * PER>();
    else wait_vm<0>();
    __builtin_amdgcn_s_barrier();
    asm volatile("" ::: "memory");
    const bool more = (s + NSTG - 1 < nstages);
    char* sb = smem + (s % NSTG) * STG;
#pragma unroll
    for (int i = 0; i < NWORK; ++i) {
      const int u = wj * NWORK + i, b = u / NKS, ks = u - b * NKS;
      bf16x8 af[MT];
#pragma unroll
      for (int mt = 0; mt < MT; ++mt) {
        u32x4 raw;
        if constexpr (AFRAG) raw = *(const u32x4*)(sb + BBYTES + (ks * (BM / 16) + rg * MT + mt) * 1024 + lane * 16);
        else raw = *(const u32x4*)(sb + BBYTES + (ks >> 1) * BM * 128 + ((rg * MT + mt) * 16 + (lane & 15)) * 128 +
                                   (((4 * (ks & 1) + g) ^ (lane & 7)) * 16));
        if constexpr (LNA) {
          const int k = kbeg + (s * NKS + ks) * 32 + 8 * g;
          const f32x4 w0 = *(const f32x4*)(lnw + k), w1 = *(const f32x4*)(lnw + k + 4);
          const f32x4 b0 = *(const f32x4*)(lnw + 576 + k), b1 = *(const f32x4*)(lnw + 576 + k + 4);
          const float nm = -mu[mt] * rs[mt];
          u32x4 o;
#pragma unroll
          for (int e = 0; e < 4; ++e) {
            const float lo = fmaf(bf2f(raw[e] & 0xffffu), rs[mt], nm);
            const float hi = fmaf(bfhi2f(raw[e]), rs[mt], nm);
            const float wl = e < 2 ? w0[2 * e] : w1[2 * e - 4], wh = e < 2 ? w0[2 * e + 1] : w1[2 * e - 3];
            const float bl = e < 2 ? b0[2 * e] : b1[2 * e - 4], bh = e < 2 ? b0[2 * e + 1] : b1[2 * e - 3];
            o[e] = pack2(fmaf(lo, wl, bl), fmaf(hi, wh, bh));
          }
          raw = o;
        }
        af[mt] = __builtin_bit_cast(bf16x8, raw);
      }
      const char* bbase = sb + (b * NKS + ks) * 9216 + brd;
#pragma unroll
      for (int t = 0; t < 9; ++t) {
        s16x4 lo = __builtin_amdgcn_ds_read_tr16_b64_v4i16((LDS_AS s16x4*)(bbase + t * 32));
        s16x4 hi = __builtin_amdgcn_ds_read_tr16_b64_v4i16((LDS_AS s16x4*)(bbase + t * 32 + 2304));
        bf16x8 bf = __builtin_shufflevector(lo, hi, 0, 1, 2, 3, 4, 5, 6, 7);
#pragma unroll
        for (int mt = 0; mt < MT; ++mt) {
          if constexpr (SWAP) acc[mt][t] = __builtin_amdgcn_mfma_f32_16x16x32_bf16(af[mt], bf, acc[mt][t], 0, 0, 0);
          else acc[mt][t] = __builtin_amdgcn_mfma_f32_16x16x32_bf16(bf, af[mt], acc[mt][t], 0, 0, 0);
        }
        constexpr int NIT = (NWORK * 9 + 1) / ISSUE_DIV;
        const int it = i * 9 + t;
        if (more && it < NIT) {
#pragma unroll
          for (int e = it * PER / NIT; e < (it + 1) * PER / NIT; ++e) issue_one(s + NSTG - 1, e);
        }
      }
    }
  }
}

template <int X, int MT>
DI void pair_reduce(f32x4 (&acc)[MT][9], f32x4 (&res)[MT / 2][9], char* smem, int w, int lane) {
  constexpr int HM = MT / 2;
  f32x4* mine = (f32x4*)smem + w * HM * 9 * 64 + lane;
  f32x4* theirs = (f32x4*)smem + (w ^ X) * HM * 9 * 64 + lane;
  const bool hi = __builtin_amdgcn_readfirstlane(w & X) != 0;
  __syncthreads();
#pragma unroll
  for (int mt = 0; mt < HM; ++mt)
#pragma unroll
    for (int t = 0; t < 9; ++t) {
      f32x4 snd, kp;
#pragma unroll
      for (int j = 0; j < 4; ++j) {
        const float lo = acc[mt][t][j], up = acc[HM + mt][t][j];
        snd[j] = hi ? lo : up;
        kp[j] = hi ? up : lo;
      }
      mine[(mt * 9 + t) * 64] = snd;
      res[mt][t] = kp;
    }
  __syncthreads();
#pragma unroll
  for (int mt = 0; mt < HM; ++mt)
#pragma unroll
    for (int t = 0; t < 9; ++t) res[mt][t] += theirs[(mt * 9 + t) * 64];
}

constexpr int K2_NSTG = 4;
constexpr int K2_STG = 2 * 9216 + 128 * 128;
constexpr int K2_SMEM = K2_NSTG * K2_STG + 2 * 576 * 4;
__global__ __launch_bounds__(512) void k_qkv(const u16* __restrict__ x, const u16* __restrict__ wqkv,
                                             const float* __restrict__ mod, const float* __restrict__ st1,
                                             u16* __restrict__ Qm, u16* __restrict__ Qt, u16* __restrict__ Km,
                                             u16* __restrict__ Kt, u16* __restrict__ Vt) {
  extern __shared__ __attribute__((aligned(16))) char smem[];
  const int tid = threadIdx.x, lane = tid & 63, w = tid >> 6;
  const int bm = blockIdx.x & 15, bn = blockIdx.x >> 4;
  float* lnw = (float*)(smem + K2_NSTG * K2_STG);
  for (int i = tid; i < 576; i += 512) {
    lnw[i] = 1.f + mod[576 + i];
    lnw[576 + i] = mod[i];
  }
  const int row0 = bm * 128 + (w & 3) * 32;
  float mu[2], rs[2];
#pragma unroll
  for (int mt = 0; mt < 2; ++mt) {
    const int r = row0 + mt * 16 + (lane & 15);
    mu[mt] = st1[r * 2];
    rs[mt] = st1[r * 2 + 1];
  }
  __syncthreads();
  f32x4 acc[2][9];
#pragma unroll
  for (int i = 0; i < 2; ++i)
#pragma unroll
    for (int j = 0; j < 9; ++j) acc[i][j] = (f32x4){0.f, 0.f, 0.f, 0.f};
  const int which = bn >> 2;
  const int mo = w >> 2;
  const int rw = row0 + mo * 16;
  const u16* bp = wqkv + bn * 144;
  f32x4 res[1][9];
  if (which == 2) {
    gemm_core2<128, 1, 2, 8, K2_NSTG, true, true, 2>(x + (size_t)bm * 128 * D_, D_, bp, bp, NQKV, 9, smem, lnw, 0, mu, rs, acc);
    pair_reduce<4, 2>(acc, res, smem, w, lane);
    const int g = lane >> 4;
#pragma unroll
    for (int t = 0; t < 9; ++t) {
      const int n = (bn & 3) * 144 + t * 16 + (lane & 15);
      const f32x4 v = res[0][t];
      const int pos = 16 * mo + 8 * (g & 1) + 4 * (g >> 1);
      *(u32x2*)(Vt + (size_t)n * S_ + row0 + pos) = (u32x2){pack2(v[0], v[1]), pack2(v[2], v[3])};
    }
    return;
  }
  gemm_core2<128, 1, 2, 8, K2_NSTG, true, false, 2>(x + (size_t)bm * 128 * D_, D_, bp, bp, NQKV, 9, smem, lnw, 0, mu, rs, acc);
  pair_reduce<4, 2>(acc, res, smem, w, lane);
  const float sc = which == 0 ? 0.23570226039551584f * 1.4426950408889634f : 1.f;
  char* tile = smem + 8 * 9 * 64 * 16;
#pragma unroll
  for (int t = 0; t < 9; ++t) {
    const f32x4 v = res[0][t];
    *(u32x2*)(tile + (rw - bm * 128 + (lane & 15)) * 296 + (t * 16 + 4 * (lane >> 4)) * 2) =
        (u32x2){pack2(v[0] * sc, v[1] * sc), pack2(v[2] * sc, v[3] * sc)};
  }
  __syncthreads();
  u16* dm = which == 0 ? Qm : Km;
  u16* dt = which == 0 ? Qt : Kt;
#pragma unroll
  for (int j = 0; j < 2; ++j) {
    const int i = tid + 512 * j, r = i & 127, hl = i >> 7;
    const unsigned* src = (const unsigned*)(tile + r * 296 + hl * 36);
    u32x4 a = {src[0], src[1], src[2], src[3]}, b = {src[4], src[5], src[6], src[7]};
    const unsigned tl = src[8];
    const size_t hr = (size_t)((bn & 3) * 8 + hl) * S_ + bm * 128 + r;
    *(u32x4*)(dm + hr * 16) = a;
    *(u32x4*)(dm + hr * 16 + 8) = b;
    *(unsigned*)(dt + hr * 2) = tl;
  }
}

constexpr int VROW = 4096 + 16;
constexpr int A_KM = 0, A_KT = 65536, A_VT = 65536 + 8192, A_END = A_VT + 20 * VROW;
constexpr int A_SMEM = A_END;
constexpr float ATT_THR = 8.f;
__global__ __launch_bounds__(1024) void k_attn(const u16* __restrict__ Qm, const u16* __restrict__ Qt,
                                               const u16* __restrict__ Km, const u16* __restrict__ Kt,
                                               const u16* __restrict__ Vt, u16* __restrict__ o) {
  extern __shared__ __attribute__((aligned(16))) char smem[];
  const int tid = threadIdx.x, lane = tid & 63;
  const int w = __builtin_amdgcn_readfirstlane(tid >> 6);
  const int bid = blockIdx.x;
  const int h = (bid & 7) * 4 + ((bid >> 3) & 3), qc = bid >> 5;
  const u16* kmg = Km + (size_t)h * S_ * 16;
  const u16* ktg = Kt + (size_t)h * S_ * 2;
  const u16* vtg = Vt + (size_t)h * DH_ * S_;
  const int r32 = lane & 31, hh = lane >> 5;
  const int qrow = qc * 256 + (w & 7) * 32 + r32;
  u32x4 qraw;
  unsigned qtw;
  {
    const u16* qp = Qm + ((size_t)h * S_ + qrow) * 16 + hh * 8;
    const u16* qtp = Qt + ((size_t)h * S_ + qrow) * 2;
    asm volatile("global_load_dwordx4 %0, %1, off" : "=&v"(qraw) : "v"(qp) : "memory");
    asm volatile("global_load_dword %0, %1, off" : "=&v"(qtw) : "v"(qtp) : "memory");
  }
#pragma unroll
  for (int c = 0; c < 4; ++c) {
    {
      const int hf = w >> 3, k0 = hf * 1024 + c * 256 + (w & 7) * 32;
      __builtin_amdgcn_global_load_lds((const void*)(kmg + k0 * 16 + lane * 8),
                                       (LDS_AS void*)(smem + A_KM + c * 16384 + w * 1024), 16, 0, 0);
    }
    {
      const int d = w;
      __builtin_amdgcn_global_load_lds((const void*)(vtg + d * S_ + hh * 1024 + c * 256 + r32 * 8),
                                       (LDS_AS void*)(smem + A_VT + d * VROW + c * 1024), 16, 0, 0);
    }
    if (w < 2) {
      const int d = 16 + w;
      __builtin_amdgcn_global_load_lds((const void*)(vtg + d * S_ + hh * 1024 + c * 256 + r32 * 8),
                                       (LDS_AS void*)(smem + A_VT + d * VROW + c * 1024), 16, 0, 0);
    } else if (w < 4) {
      const int hf = w - 2;
      __builtin_amdgcn_global_load_lds((const void*)(ktg + (hf * 1024 + c * 256) * 2 + lane * 8),
                                       (LDS_AS void*)(smem + A_KT + c * 2048 + hf * 1024), 16, 0, 0);
    }
  }
  if (tid < 512) {
    const int rr = 18 + (tid >> 8), cidx = tid & 255;
    const unsigned val = rr == 18 ? 0x3f803f80u : 0u;
    *(u32x4*)(smem + A_VT + rr * VROW + cidx * 16) = (u32x4){val, val, val, val};
  }
  const int khalf = w >> 3;

  const char* kmp = smem + A_KM + khalf * 8192 + r32 * 32 + hh * 16;
  const char* ktp = hh == 0 ? (smem + A_KT + khalf * 1024 + r32 * 4) : (smem + A_VT + 19 * VROW);
  const int ktc = hh == 0 ? 2048 : 0, ktt = hh == 0 ? 128 : 0;
  const int vrow = r32 < 19 ? r32 : 19;
  const char* vp = smem + A_VT + vrow * VROW + khalf * 512 + hh * 16;

  bf16x8 qf0, qf1;
  f32x16 oacc, negm;
#pragma unroll
  for (int i = 0; i < 16; ++i) { negm[i] = 0.f; oacc[i] = 0.f; }
  float m = 0.f;
  auto qk = [&](int tt) -> f32x16 {
    const int c = tt >> 3, t8 = tt & 7;
    bf16x8 kf0 = *(const bf16x8*)(kmp + c * 16384 + t8 * 1024);
    unsigned kt = *(const unsigned*)(ktp + c * ktc + t8 * ktt);
    bf16x8 kf1 = __builtin_bit_cast(bf16x8, (u32x4){kt, 0u, 0u, 0u});
    f32x16 s = __builtin_amdgcn_mfma_f32_32x32x16_bf16(kf0, qf0, negm, 0, 0, 0);
    return __builtin_amdgcn_mfma_f32_32x32x16_bf16(kf1, qf1, s, 0, 0, 0);
  };
  auto pv = [&](int tt, const f32x16& s) {
    const int c = tt >> 3, t8 = tt & 7;
    u32x4 p0, p1;
#pragma unroll
    for (int e = 0; e < 4; ++e) {
      p0[e] = pack2(__builtin_amdgcn_exp2f(s[2 * e]), __builtin_amdgcn_exp2f(s[2 * e + 1]));
      p1[e] = pack2(__builtin_amdgcn_exp2f(s[8 + 2 * e]), __builtin_amdgcn_exp2f(s[8 + 2 * e + 1]));
    }
    bf16x8 vf0 = *(const bf16x8*)(vp + c * 1024 + t8 * 64);
    bf16x8 vf1 = *(const bf16x8*)(vp + c * 1024 + t8 * 64 + 32);
    oacc = __builtin_amdgcn_mfma_f32_32x32x16_bf16(vf0, __builtin_bit_cast(bf16x8, p0), oacc, 0, 0, 0);
    oacc = __builtin_amdgcn_mfma_f32_32x32x16_bf16(vf1, __builtin_bit_cast(bf16x8, p1), oacc, 0, 0, 0);
  };
  auto fixmax = [&](f32x16& sc, f32x16& sn, bool first, bool has_next) {
    float tm = fmaxf(fmaxf(sc[0], sc[1]), sc[2]);
#pragma unroll
    for (int i = 3; i < 15; i += 2) tm = fmaxf(fmaxf(tm, sc[i]), sc[i + 1]);
    tm = fmaxf(tm, sc[15]);
    if (first || __any(tm > ATT_THR)) {
      float d = fmaxf(tm, __shfl_xor(tm, 32));
      if (!first) d = fmaxf(d, 0.f);
      m += d;
      const float al = __builtin_amdgcn_exp2f(-d);
#pragma unroll
      for (int i = 0; i < 16; ++i) {
        negm[i] = -m;
        sc[i] -= d;
        if (has_next) sn[i] -= d;
        if (!first) oacc[i] *= al;
      }
    }
  };
  const int nper = (w < 4) ? 3 : 2;
  f32x16 sA, sB;
#pragma unroll
  for (int c = 0; c < 4; ++c) {
    if (nper == 3) {
      if (c == 0) wait_vm<9>(); else if (c == 1) wait_vm<6>(); else if (c == 2) wait_vm<3>(); else wait_vm<0>();
    } else {
      if (c == 0) wait_vm<6>(); else if (c == 1) wait_vm<4>(); else if (c == 2) wait_vm<2>(); else wait_vm<0>();
    }
    asm volatile("s_waitcnt lgkmcnt(0)" ::: "memory");
    __builtin_amdgcn_s_barrier();
    asm volatile("" ::: "memory");
    if (c == 0) {
      asm volatile("" : "+v"(qraw), "+v"(qtw));
      qf0 = __builtin_bit_cast(bf16x8, qraw);
      qf1 = __builtin_bit_cast(bf16x8, (u32x4){hh == 0 ? qtw : 0u, 0u, 0u, 0u});
    }
    sA = qk(8 * c);
    if (c == 0) fixmax(sA, sB, true, false);
#pragma unroll 1
    for (int t = 8 * c; t < 8 * c + 8; t += 2) {
      sB = qk(t + 1);
      pv(t, sA);
      if (t + 2 < 8 * c + 8) sA = qk(t + 2);
      pv(t + 1, sB);
    }
  }
  {
    bool bad = false;
#pragma unroll
    for (int i = 0; i < 11; ++i) bad |= !(fabsf(oacc[i]) < 1.0e37f);
    if (__any(bad)) {
#pragma unroll
      for (int i = 0; i < 16; ++i) { negm[i] = 0.f; oacc[i] = 0.f; }
      m = 0.f;
      sA = qk(0);
#pragma unroll 1
      for (int t = 0; t < 32; t += 2) {
        sB = qk(t + 1);
        fixmax(sA, sB, t == 0, true);
        pv(t, sA);
        const bool nx = (t + 2 < 32);
        if (nx) sA = qk(t + 2);
        fixmax(sB, sA, false, nx);
        pv(t + 1, sB);
      }
    }
  }
  __syncthreads();
  float* cb = (float*)(smem + A_KM) + (w & 7) * 64 * 17;
  if (khalf == 1) {
#pragma unroll
    for (int i = 0; i < 11; ++i) cb[i * 64 + lane] = oacc[i];
    cb[11 * 64 + lane] = m;
  }
  __syncthreads();
  if (khalf == 1) return;
  {
    const float m2 = cb[11 * 64 + lane];
    const float mm = fmaxf(m, m2);
    const float a1 = __builtin_amdgcn_exp2f(m - mm), a2 = __builtin_amdgcn_exp2f(m2 - mm);
#pragma unroll
    for (int i = 0; i < 11; ++i) oacc[i] = oacc[i] * a1 + cb[i * 64 + lane] * a2;
  }
  const float l = __shfl(oacc[10], r32);
  const float inv = 1.f / l;
  u16* op = o + (size_t)qrow * D_ + h * DH_;
  {
    u32x2 a = {pack2(oacc[0] * inv, oacc[1] * inv), pack2(oacc[2] * inv, oacc[3] * inv)};
    u32x2 b = {pack2(oacc[4] * inv, oacc[5] * inv), pack2(oacc[6] * inv, oacc[7] * inv)};
    unsigned* p32 = (unsigned*)(op + hh * 4);
    p32[0] = a[0]; p32[1] = a[1];
    p32[4] = b[0]; p32[5] = b[1];
    if (hh == 0) *(unsigned*)(op + 16) = pack2(oacc[8] * inv, oacc[9] * inv);
  }
}

constexpr int K4_NSTG = 5;
constexpr int K4_STG = 2 * 9216 + 64 * 128;
constexpr int K4_SMEM = K4_NSTG * K4_STG;
__global__ __launch_bounds__(256) void k_wo(const u16* __restrict__ o, const u16* __restrict__ wo,
                                            const u16* __restrict__ x, const float* __restrict__ mod,
                                            u16* __restrict__ x1) {
  extern __shared__ __attribute__((aligned(16))) char smem[];
  const int tid = threadIdx.x, lane = tid & 63, w = tid >> 6;
  const int bm = blockIdx.x & 31, bn = blockIdx.x >> 5;
  float mu[2] = {0.f, 0.f}, rs[2] = {0.f, 0.f};
  f32x4 acc[2][9];
#pragma unroll
  for (int i = 0; i < 2; ++i)
#pragma unroll
    for (int j = 0; j < 9; ++j) acc[i][j] = (f32x4){0.f, 0.f, 0.f, 0.f};
  const u16* bp = wo + bn * 144;
  gemm_core2<64, 1, 2, 4, K4_NSTG, false, false, 2>(o + (size_t)bm * 64 * D_, D_, bp, bp, D_, 9, smem, nullptr, 0, mu, rs, acc);
  f32x4 res[1][9];
  pair_reduce<2, 2>(acc, res, smem, w, lane);
  const int r = bm * 64 + (w & 1) * 32 + (w >> 1) * 16 + (lane & 15);
#pragma unroll
  for (int t = 0; t < 9; ++t) {
    const f32x4 v = res[0][t];
    const int n = bn * 144 + t * 16 + 4 * (lane >> 4);
    const u32x2 xv = *(const u32x2*)(x + (size_t)r * D_ + n);
    const f32x4 gv = *(const f32x4*)(mod + 2 * 576 + n);
    const float y0 = bf2f(xv[0] & 0xffffu) + rbf(gv[0] * rbf(v[0]));
    const float y1 = bfhi2f(xv[0]) + rbf(gv[1] * rbf(v[1]));
    const float y2 = bf2f(xv[1] & 0xffffu) + rbf(gv[2] * rbf(v[2]));
    const float y3 = bfhi2f(xv[1]) + rbf(gv[3] * rbf(v[3]));
    *(u32x2*)(x1 + (size_t)r * D_ + n) = (u32x2){pack2(y0, y1), pack2(y2, y3)};
  }
}

__global__ __launch_bounds__(64) void k_ln2(const u16* __restrict__ x1, const float* __restrict__ mod, u16* __restrict__ h2) {
  const int lane = threadIdx.x;
  const int row = blockIdx.x * 8 + (lane >> 3), j = lane & 7;
  const u16* xr = x1 + (size_t)row * D_;
  u32x4 q[9];
#pragma unroll
  for (int i = 0; i < 9; ++i) q[i] = *(const u32x4*)(xr + (j + 8 * i) * 8);
  float v[72];
#pragma unroll
  for (int i = 0; i < 9; ++i) {
#pragma unroll
    for (int e = 0; e < 4; ++e) {
      v[i * 8 + 2 * e] = bf2f(q[i][e] & 0xffffu);
      v[i * 8 + 2 * e + 1] = bfhi2f(q[i][e]);
    }
  }
  float s = 0.f;
#pragma unroll
  for (int i = 0; i < 72; ++i) s += v[i];
  s += __shfl_xor(s, 1); s += __shfl_xor(s, 2); s += __shfl_xor(s, 4);
  const float mean = s * (1.f / 576.f);
  float q2 = 0.f;
#pragma unroll
  for (int i = 0; i < 72; ++i) { const float d = v[i] - mean; q2 += d * d; }
  q2 += __shfl_xor(q2, 1); q2 += __shfl_xor(q2, 2); q2 += __shfl_xor(q2, 4);
  const float rstd = 1.f / sqrtf(q2 * (1.f / 576.f) + 1e-5f);
#pragma unroll
  for (int i = 0; i < 9; ++i) {
    const int k = (j + 8 * i) * 8;
    const f32x4 c0 = *(const f32x4*)(mod + 4 * 576 + k), c1 = *(const f32x4*)(mod + 4 * 576 + k + 4);
    const f32x4 b0 = *(const f32x4*)(mod + 3 * 576 + k), b1 = *(const f32x4*)(mod + 3 * 576 + k + 4);
    u32x4 o;
#pragma unroll
    for (int e = 0; e < 4; ++e) {
      const float cl = e < 2 ? c0[2 * e] : c1[2 * e - 4], ch = e < 2 ? c0[2 * e + 1] : c1[2 * e - 3];
      const float bl = e < 2 ? b0[2 * e] : b1[2 * e - 4], bh = e < 2 ? b0[2 * e + 1] : b1[2 * e - 3];
      o[e] = pack2(fmaf((v[i * 8 + 2 * e] - mean) * rstd, 1.f + cl, bl), fmaf((v[i * 8 + 2 * e + 1] - mean) * rstd, 1.f + ch, bh));
    }
    const int c = j + 8 * i;
    *(u32x4*)(h2 + ((size_t)((row >> 4) * 18 + (c >> 2)) * 64 + (c & 3) * 16 + (row & 15)) * 8) = o;
  }
}

constexpr int K5_NSTG = 4;
constexpr int K5_STG = 2 * 9216 + 16 * 1024;
constexpr int K5_SMEM = K5_NSTG * K5_STG;
__global__ __launch_bounds__(512) void k_gateup(const u16* __restrict__ h2, const u16* __restrict__ wg,
                                                const u16* __restrict__ wu, u16* __restrict__ hh) {
  extern __shared__ __attribute__((aligned(16))) char smem[];
  const int tid = threadIdx.x, lane = tid & 63, w = tid >> 6;
  const int bn = (blockIdx.x & 7) * 4 + ((blockIdx.x >> 3) & 3), bm = blockIdx.x >> 5;
  const int row0 = bm * 256 + (w & 3) * 64;
  float mu[4] = {0.f, 0.f, 0.f, 0.f}, rs[4] = {0.f, 0.f, 0.f, 0.f};
  f32x4 acc[4][9];
#pragma unroll
  for (int i = 0; i < 4; ++i)
#pragma unroll
    for (int j = 0; j < 9; ++j) acc[i][j] = (f32x4){0.f, 0.f, 0.f, 0.f};
  gemm_core2<256, 2, 1, 8, K5_NSTG, false, false, 4, true>(h2 + (size_t)bm * 16 * 18 * 512, 18, wg + bn * 144, wu + bn * 144, F_,
                                                           18, smem, nullptr, 0, mu, rs, acc);
  __syncthreads();
  unsigned* ex = (unsigned*)smem + (w & 3) * 72 * 64 + lane;
  if (w >= 4) {
#pragma unroll
    for (int mt = 0; mt < 4; ++mt)
#pragma unroll
      for (int t = 0; t < 9; ++t) {
        ex[((mt * 9 + t) * 2) * 64] = pack2(acc[mt][t][0], acc[mt][t][1]);
        ex[((mt * 9 + t) * 2 + 1) * 64] = pack2(acc[mt][t][2], acc[mt][t][3]);
      }
  }
  __syncthreads();
  if (w >= 4) return;
#pragma unroll
  for (int mt = 0; mt < 4; ++mt) {
    const int r = row0 + mt * 16 + (lane & 15);
#pragma unroll
    for (int t = 0; t < 9; ++t) {
      const int n = bn * 144 + t * 16 + 4 * (lane >> 4);
      const unsigned u0 = ex[((mt * 9 + t) * 2) * 64], u1 = ex[((mt * 9 + t) * 2 + 1) * 64];
      const float y0 = rbf(acc[mt][t][0]) * bf2f(u0 & 0xffffu), y1 = rbf(acc[mt][t][1]) * bfhi2f(u0);
      const float y2 = rbf(acc[mt][t][2]) * bf2f(u1 & 0xffffu), y3 = rbf(acc[mt][t][3]) * bfhi2f(u1);
      ST_OUT((u32x2*)(hh + ((size_t)((r >> 4) * 144 + (n >> 5)) * 64 + ((n >> 3) & 3) * 16 + (r & 15)) * 8 + (n & 7)),
             ((u32x2){pack2(y0, y1), pack2(y2, y3)}));
    }
  }
}

constexpr int K6_NSTG = 4;
constexpr int K6_STG = 2 * 9216 + 8 * 2 * 1024;
constexpr int K6_SMEM = K6_NSTG * K6_STG;
__global__ __launch_bounds__(512) void k_down(const u16* __restrict__ hh, const u16* __restrict__ wd,
                                              float* __restrict__ part) {
  extern __shared__ __attribute__((aligned(16))) char smem[];
  const int tid = threadIdx.x, lane = tid & 63, w = tid >> 6;
  const int xcd = blockIdx.x & 7, idx = blockIdx.x >> 3;
  const int sk = xcd >> 1, bn = idx & 3, bm = (xcd & 1) * 8 + (idx >> 2);
  float mu[2] = {0.f, 0.f}, rs[2] = {0.f, 0.f};
  f32x4 acc[2][9];
#pragma unroll
  for (int i = 0; i < 2; ++i)
#pragma unroll
    for (int j = 0; j < 9; ++j) acc[i][j] = (f32x4){0.f, 0.f, 0.f, 0.f};
  const u16* bp = wd + (size_t)sk * 1152 * D_ + bn * 144;
  gemm_core2<128, 1, 2, 8, K6_NSTG, false, false, 2, true>(hh + ((size_t)bm * 8 * 144 + sk * 36) * 512, 144, bp, bp, D_, 18, smem,
                                                           nullptr, 0, mu, rs, acc);
  f32x4 res[1][9];
  pair_reduce<4, 2>(acc, res, smem, w, lane);
  float* pp = part + (size_t)sk * S_ * D_;
  const int r = bm * 128 + (w & 3) * 32 + (w >> 2) * 16 + (lane & 15);
#pragma unroll
  for (int t = 0; t < 9; ++t) {
    const int n = bn * 144 + t * 16 + 4 * (lane >> 4);
    ST_OUT((f32x4*)(pp + (size_t)r * D_ + n), res[0][t]);
  }
}

__global__ __launch_bounds__(256) void k_final(const float* __restrict__ part, const u16* __restrict__ x1,
                                               const float* __restrict__ mod, u16* __restrict__ out) {
  const int i = blockIdx.x * 256 + threadIdx.x;
  const int e = i * 8;
  const int n = e % D_;
  f32x4 a0 = *(const f32x4*)(part + e), a1 = *(const f32x4*)(part + e + 4);
#pragma unroll
  for (int s = 1; s < 4; ++s) {
    a0 += *(const f32x4*)(part + (size_t)s * S_ * D_ + e);
    a1 += *(const f32x4*)(part + (size_t)s * S_ * D_ + e + 4);
  }
  const u32x4 xv = *(const u32x4*)(x1 + e);
  const f32x4 g0 = *(const f32x4*)(mod + 5 * 576 + n), g1 = *(const f32x4*)(mod + 5 * 576 + n + 4);
  u32x4 r;
  r[0] = pack2(bf2f(xv[0] & 0xffffu) + rbf(g0[0] * rbf(a0[0])), bfhi2f(xv[0]) + rbf(g0[1] * rbf(a0[1])));
  r[1] = pack2(bf2f(xv[1] & 0xffffu) + rbf(g0[2] * rbf(a0[2])), bfhi2f(xv[1]) + rbf(g0[3] * rbf(a0[3])));
  r[2] = pack2(bf2f(xv[2] & 0xffffu) + rbf(g1[0] * rbf(a1[0])), bfhi2f(xv[2]) + rbf(g1[1] * rbf(a1[1])));
  r[3] = pack2(bf2f(xv[3] & 0xffffu) + rbf(g1[2] * rbf(a1[2])), bfhi2f(xv[3]) + rbf(g1[3] * rbf(a1[3])));
  *(u32x4*)(out + e) = r;
}

extern "C" void kernel_launch(void* const* d_in, const int* in_sizes, int n_in, void* d_out, int out_size,
                              void* d_ws, size_t ws_size, hipStream_t stream) {
  const u16* x = (const u16*)d_in[0];
  const u16* wqkv = (const u16*)d_in[1];
  const u16* wo = (const u16*)d_in[2];
  const u16* wg = (const u16*)d_in[4];
  const u16* wu = (const u16*)d_in[5];
  const u16* wd = (const u16*)d_in[6];
  const u16* c = (const u16*)d_in[7];
  const u16* wmod = (const u16*)d_in[8];
  char* ws = (char*)d_ws;
  float* mod = (float*)(ws + WS_MOD);
  float* st1 = (float*)(ws + WS_ST1);
  u16* Qm = (u16*)(ws + WS_QM);
  u16* Qt = (u16*)(ws + WS_QT);
  u16* Km = (u16*)(ws + WS_KM);
  u16* Kt = (u16*)(ws + WS_KT);
  u16* Vt = (u16*)(ws + WS_VT);
  u16* o = (u16*)(ws + WS_O);
  u16* x1 = (u16*)(ws + WS_X1);
  float* st2 = (float*)(ws + WS_ST2);
  u16* hh = (u16*)(ws + WS_HH);
  u16* h2 = (u16*)(ws + WS_QM);
  float* part = (float*)(ws + WS_PART);

  (void)hipFuncSetAttribute((const void*)k_qkv, hipFuncAttributeMaxDynamicSharedMemorySize, K2_SMEM);
  (void)hipFuncSetAttribute((const void*)k_attn, hipFuncAttributeMaxDynamicSharedMemorySize, A_SMEM);
  (void)hipFuncSetAttribute((const void*)k_wo, hipFuncAttributeMaxDynamicSharedMemorySize, K4_SMEM);
  (void)hipFuncSetAttribute((const void*)k_gateup, hipFuncAttributeMaxDynamicSharedMemorySize, K5_SMEM);
  (void)hipFuncSetAttribute((const void*)k_down, hipFuncAttributeMaxDynamicSharedMemorySize, K6_SMEM);

  k_prep<<<108 + 64, 256, 0, stream>>>(c, wmod, x, mod, st1);
  k_qkv<<<16 * 12, 512, K2_SMEM, stream>>>(x, wqkv, mod, st1, Qm, Qt, Km, Kt, Vt);
  k_attn<<<256, 1024, A_SMEM, stream>>>(Qm, Qt, Km, Kt, Vt, o);
  k_wo<<<32 * 4, 256, K4_SMEM, stream>>>(o, wo, x, mod, x1);
  k_ln2<<<256, 64, 0, stream>>>(x1, mod, h2);
  k_gateup<<<8 * 32, 512, K5_SMEM, stream>>>(h2, wg, wu, hh);
  k_down<<<16 * 4 * 4, 512, K6_SMEM, stream>>>(hh, wd, part);
  k_final<<<S_ * D_ / 8 / 256, 256, 0, stream>>>(part, x1, mod, (u16*)d_out);
}
```

```cpp
#include <hip/hip_runtime.h>
#include <hip/hip_bf16.h>
#include <stdint.h>

typedef short bf16x8 __attribute__((ext_vector_type(8)));
typedef short s16x4 __attribute__((ext_vector_type(4)));
typedef float f32x4 __attribute__((ext_vector_type(4)));
typedef float f32x16 __attribute__((ext_vector_type(16)));
typedef unsigned u32x4 __attribute__((ext_vector_type(4)));
typedef unsigned u32x2 __attribute__((ext_vector_type(2)));
typedef unsigned short u16;
typedef __bf16 bf2_t __attribute__((ext_vector_type(2)));
typedef float f2_t __attribute__((ext_vector_type(2)));

#define DI __device__ __forceinline__
#define ST_OUT(p, v) (*(p) = (v))
#define LDS_AS __attribute__((address_space(3)))

constexpr int S_ = 2048, D_ = 576, H_ = 32, DH_ = 18, F_ = 4608;
constexpr int NQKV = 3 * D_;

DI float bf2f(unsigned v) { return __uint_as_float(v << 16); }
DI float bfhi2f(unsigned v) { return __uint_as_float(v & 0xffff0000u); }
DI unsigned pack2(float lo, float hi) {
  bf2_t r = __builtin_convertvector((f2_t){lo, hi}, bf2_t);
  return __builtin_bit_cast(unsigned, r);
}
DI float rbf(float x) { return bf2f(pack2(x, 0.f) & 0xffffu); }

constexpr size_t WS_MOD = 0;
constexpr size_t WS_ST1 = 16384;
constexpr size_t WS_QM = 32768;
constexpr size_t WS_QT = WS_QM + (size_t)H_ * S_ * 16 * 2;
constexpr size_t WS_KM = WS_QT + (size_t)H_ * S_ * 2 * 2;
constexpr size_t WS_KT = WS_KM + (size_t)H_ * S_ * 16 * 2;
constexpr size_t WS_VT = WS_KT + (size_t)H_ * S_ * 2 * 2;
constexpr size_t WS_O = WS_VT + (size_t)H_ * DH_ * S_ * 2;
constexpr size_t WS_X1 = WS_O + (size_t)S_ * D_ * 2;
constexpr size_t WS_ST2 = WS_X1 + (size_t)S_ * D_ * 2;
constexpr size_t WS_HH = WS_ST2 + (size_t)S_ * 8 * 4;
constexpr size_t WS_PART = WS_HH + (size_t)S_ * F_ * 2;
constexpr size_t WS_WG = WS_PART + (size_t)4 * S_ * D_ * 4;
constexpr size_t WS_WU = WS_WG + (size_t)D_ * F_ * 2;
constexpr size_t WS_CV = WS_WU + (size_t)D_ * F_ * 2;
constexpr size_t WS_END = WS_CV + (size_t)4 * F_ * 4;

__global__ __launch_bounds__(256) void k_prep(const u16* __restrict__ c, const u16* __restrict__ wmod,
                                              const u16* __restrict__ x, float* __restrict__ mod,
                                              float* __restrict__ st1) {
  const int tid = threadIdx.x;
  if (blockIdx.x < 108) {
    __shared__ float red[64][33];
    const int tx = tid & 3, ty = tid >> 2;
    const int col0 = blockIdx.x * 32 + tx * 8;
    float acc[8];
#pragma unroll
    for (int j = 0; j < 8; ++j) acc[j] = 0.f;
    u32x4 w[9];
#pragma unroll
    for (int i = 0; i < 9; ++i) w[i] = *(const u32x4*)(wmod + (size_t)(ty + 64 * i) * 3456 + col0);
#pragma unroll
    for (int i = 0; i < 9; ++i) {
      const float cv = bf2f(c[ty + 64 * i]);
      const float s = rbf(cv / (1.f + __expf(-cv)));
#pragma unroll
      for (int j = 0; j < 4; ++j) {
        acc[2 * j] += s * bf2f(w[i][j] & 0xffffu);
        acc[2 * j + 1] += s * bfhi2f(w[i][j]);
      }
    }
#pragma unroll
    for (int j = 0; j < 8; ++j) red[ty][tx * 8 + j] = acc[j];
    __syncthreads();
    if (tid < 32) {
      float s = 0.f;
      for (int r = 0; r < 64; ++r) s += red[r][tid];
      mod[blockIdx.x * 32 + tid] = rbf(s);
    }
  } else {
    const int lane = tid & 63, w = tid >> 6;
    const int row = (blockIdx.x - 108) * 32 + w * 8 + (lane >> 3), j = lane & 7;
    const u16* xr = x + (size_t)row * D_;
    float v[72];
#pragma unroll
    for (int i = 0; i < 9; ++i) {
      u32x4 q = *(const u32x4*)(xr + (j + 8 * i) * 8);
#pragma unroll
      for (int e = 0; e < 4; ++e) {
        v[i * 8 + 2 * e] = bf2f(q[e] & 0xffffu);
        v[i * 8 + 2 * e + 1] = bfhi2f(q[e]);
      }
    }
    float s = 0.f;
#pragma unroll
    for (int i = 0; i < 72; ++i) s += v[i];
    s += __shfl_xor(s, 1); s += __shfl_xor(s, 2); s += __shfl_xor(s, 4);
    const float mean = s * (1.f / 576.f);
    float q2 = 0.f;
#pragma unroll
    for (int i = 0; i < 72; ++i) { const float d = v[i] - mean; q2 += d * d; }
    q2 += __shfl_xor(q2, 1); q2 += __shfl_xor(q2, 2); q2 += __shfl_xor(q2, 4);
    if (j == 0) {
      st1[row * 2] = mean;
      st1[row * 2 + 1] = 1.f / sqrtf(q2 * (1.f / 576.f) + 1e-5f);
    }
  }
}

#define WAIT_VM(n) asm volatile("s_waitcnt vmcnt(" #n ")" ::: "memory")

template <int N> DI void wait_vm() { asm volatile("s_waitcnt vmcnt(%0)" ::"n"(N) : "memory"); }

#define ISSUE_DIV 2
template <int BM, int NB, int NKS, int NW, int NSTG, bool LNA, bool SWAP, int MT = 4, bool AFRAG = false>
DI void gemm_core2(const u16* __restrict__ Ag,
                   int lda, const u16* __restrict__ B0, const u16* __restrict__ B1,
                   int ldb, int nstages, char* smem, const float* lnw, int kbeg, const float (&mu)[MT],
                   const float (&rs)[MT], f32x4 (&acc)[MT][9]) {
  constexpr int RG = BM / (16 * MT);
  constexpr int NWORK = NB * NKS * RG / NW;
  constexpr int BBYTES = NB * NKS * 9216;
  constexpr int STG = BBYTES + (BM / 16) * NKS * 1024;
  constexpr int APW = (BM / 16) * NKS / NW;
  constexpr int BPW = NB * NKS * 576 / NW;
  constexpr int NBI = (BPW + 63) / 64;
  constexpr int BREM = BPW - (NBI - 1) * 64;
  constexpr int PER = APW + NBI;
  const int tid = threadIdx.x, lane = tid & 63;
  const int w = __builtin_amdgcn_readfirstlane(tid >> 6);
  const int rg = w % RG, wj = w / RG;
  const u16* Bw = ((BPW * w) / (576 * NKS) == 0) ? B0 : B1;
  unsigned bsrc[NBI];
#pragma unroll
  for (int i = 0; i < NBI; ++i) {
    const int c = BPW * w + 64 * i + (lane < BREM || i < NBI - 1 ? lane : 0);
    const int st = c / 576, cin = c - st * 576;
    const int ks = st % NKS;
    const int pos = cin / 18, cc = cin - pos * 18;
    const int r = (pos & 16) | ((pos & 1) << 3) | ((pos >> 1) & 7);
    bsrc[i] = (unsigned)((ks * 32 + r) * ldb + cc * 8);
  }
  unsigned asrc[APW];
#pragma unroll
  for (int i = 0; i < APW; ++i) {
    if constexpr (AFRAG) {
      const int blk = w * APW + i, ks = blk / (BM / 16), rt = blk - ks * (BM / 16);
      asrc[i] = (unsigned)(((rt * lda + ks) * 64 + lane) * 8);
    } else {
      const int blk = w * APW + i, kp = blk / (BM / 8), r8 = blk - kp * (BM / 8);
      asrc[i] = (unsigned)((r8 * 8 + (lane >> 3)) * lda + kp * 64 + (((lane & 7) ^ (lane >> 3)) * 8));
    }
  }
  auto issue_one = [&](int s, int i) {
    char* sb = smem + (s % NSTG) * STG;
    if (i < APW) {
      __builtin_amdgcn_global_load_lds((const void*)(Ag + s * NKS * (AFRAG ? 512 : 32) + asrc[i]), (LDS_AS void*)(sb + BBYTES + (w * APW + i) * 1024), 16, 0, 0);
    } else {
      const int j = i - APW;
      const u16* bb = Bw + (size_t)s * NKS * 32 * ldb;
      if (j < NBI - 1 || BREM == 64 || lane < BREM)
        __builtin_amdgcn_global_load_lds((const void*)(bb + bsrc[j]), (LDS_AS void*)(sb + (BPW * w + 64 * j) * 16), 16, 0, 0);
    }
  };
#pragma unroll
  for (int s = 0; s < NSTG - 1; ++s)
    if (s < nstages) {
#pragma unroll
      for (int i = 0; i < PER; ++i) issue_one(s, i);
    }
  const int g = lane >> 4, q = (lane & 15) >> 2, p = lane & 3;
  const int brd = (16 * (g >> 1) + 2 * q + (g & 1)) * 288 + 8 * p;
  for (int s = 0; s < nstages; ++s) {
    if (s + NSTG - 2 < nstages) wait_vm<(NSTG - 2) * PER>();
    else wait_vm<0>();
    __builtin_amdgcn_s_barrier();
    asm volatile("" ::: "memory");
    const bool more = (s + NSTG - 1 < nstages);
    char* sb = smem + (s % NSTG) * STG;
#pragma unroll
    for (int i = 0; i < NWORK; ++i) {
      const int u = wj * NWORK + i, b = u / NKS, ks = u - b * NKS;
      bf16x8 af[MT];
#pragma unroll
      for (int mt = 0; mt < MT; ++mt) {
        u32x4 raw;
        if constexpr (AFRAG) raw = *(const u32x4*)(sb + BBYTES + (ks * (BM / 16) + rg * MT + mt) * 1024 + lane * 16);
        else raw = *(const u32x4*)(sb + BBYTES + (ks >> 1) * BM * 128 + ((rg * MT + mt) * 16 + (lane & 15)) * 128 +
                                   (((4 * (ks & 1) + g) ^ (lane & 7)) * 16));
        if constexpr (LNA) {
          const int k = kbeg + (s * NKS + ks) * 32 + 8 * g;
          const f32x4 w0 = *(const f32x4*)(lnw + k), w1 = *(const f32x4*)(lnw + k + 4);
          const f32x4 b0 = *(const f32x4*)(lnw + 576 + k), b1 = *(const f32x4*)(lnw + 576 + k + 4);
          const float nm = -mu[mt] * rs[mt];
          u32x4 o;
#pragma unroll
          for (int e = 0; e < 4; ++e) {
            const float lo = fmaf(bf2f(raw[e] & 0xffffu), rs[mt], nm);
            const float hi = fmaf(bfhi2f(raw[e]), rs[mt], nm);
            const float wl = e < 2 ? w0[2 * e] : w1[2 * e - 4], wh = e < 2 ? w0[2 * e + 1] : w1[2 * e - 3];
            const float bl = e < 2 ? b0[2 * e] : b1[2 * e - 4], bh = e < 2 ? b0[2 * e + 1] : b1[2 * e - 3];
            o[e] = pack2(fmaf(lo, wl, bl), fmaf(hi, wh, bh));
          }
          raw = o;
        }
        af[mt] = __builtin_bit_cast(bf16x8, raw);
      }
      const char* bbase = sb + (b * NKS + ks) * 9216 + brd;
#pragma unroll
      for (int t = 0; t < 9; ++t) {
        s16x4 lo = __builtin_amdgcn_ds_read_tr16_b64_v4i16((LDS_AS s16x4*)(bbase + t * 32));
        s16x4 hi = __builtin_amdgcn_ds_read_tr16_b64_v4i16((LDS_AS s16x4*)(bbase + t * 32 + 2304));
        bf16x8 bf = __builtin_shufflevector(lo, hi, 0, 1, 2, 3, 4, 5, 6, 7);
#pragma unroll
        for (int mt = 0; mt < MT; ++mt) {
          if constexpr (SWAP) acc[mt][t] = __builtin_amdgcn_mfma_f32_16x16x32_bf16(af[mt], bf, acc[mt][t], 0, 0, 0);
          else acc[mt][t] = __builtin_amdgcn_mfma_f32_16x16x32_bf16(bf, af[mt], acc[mt][t], 0, 0, 0);
        }
        constexpr int NIT = (NWORK * 9 + 1) / ISSUE_DIV;
        const int it = i * 9 + t;
        if (more && it < NIT) {
#pragma unroll
          for (int e = it * PER / NIT; e < (it + 1) * PER / NIT; ++e) issue_one(s + NSTG - 1, e);
        }
      }
    }
  }
}

template <int X, int MT>
DI void pair_reduce(f32x4 (&acc)[MT][9], f32x4 (&res)[MT / 2][9], char* smem, int w, int lane) {
  constexpr int HM = MT / 2;
  f32x4* mine = (f32x4*)smem + w * HM * 9 * 64 + lane;
  f32x4* theirs = (f32x4*)smem + (w ^ X) * HM * 9 * 64 + lane;
  const bool hi = __builtin_amdgcn_readfirstlane(w & X) != 0;
  __syncthreads();
#pragma unroll
  for (int mt = 0; mt < HM; ++mt)
#pragma unroll
    for (int t = 0; t < 9; ++t) {
      f32x4 snd, kp;
#pragma unroll
      for (int j = 0; j < 4; ++j) {
        const float lo = acc[mt][t][j], up = acc[HM + mt][t][j];
        snd[j] = hi ? lo : up;
        kp[j] = hi ? up : lo;
      }
      mine[(mt * 9 + t) * 64] = snd;
      res[mt][t] = kp;
    }
  __syncthreads();
#pragma unroll
  for (int mt = 0; mt < HM; ++mt)
#pragma unroll
    for (int t = 0; t < 9; ++t) res[mt][t] += theirs[(mt * 9 + t) * 64];
}

constexpr int K2_NSTG = 4;
constexpr int K2_STG = 2 * 9216 + 128 * 128;
constexpr int K2_SMEM = K2_NSTG * K2_STG + 2 * 576 * 4;
__global__ __launch_bounds__(512) void k_qkv(const u16* __restrict__ x, const u16* __restrict__ wqkv,
                                             const float* __restrict__ mod, const float* __restrict__ st1,
                                             u16* __restrict__ Qm, u16* __restrict__ Qt, u16* __restrict__ Km,
                                             u16* __restrict__ Kt, u16* __restrict__ Vt, const u16* __restrict__ wg,
                                             const u16* __restrict__ wu, float* __restrict__ cv) {
  extern __shared__ __attribute__((aligned(16))) char smem[];
  const int tid = threadIdx.x, lane = tid & 63, w = tid >> 6;
  if (blockIdx.x >= 192) {
    const int j = blockIdx.x - 192, mtx = j >> 5, col0 = (j & 31) * 144;
    const u16* src = (mtx ? wu : wg) + col0;
    float* red = (float*)smem;
    if (tid < 432) {
      const int cc = tid % 18, rg = tid / 18;
      float c1[8], c2[8];
#pragma unroll
      for (int e = 0; e < 8; ++e) { c1[e] = 0.f; c2[e] = 0.f; }
      u32x4 v[24];
#pragma unroll
      for (int i = 0; i < 24; ++i) v[i] = *(const u32x4*)(src + (size_t)(rg + 24 * i) * F_ + cc * 8);
#pragma unroll
      for (int i = 0; i < 24; ++i) {
        const int k = rg + 24 * i;
        const float wk = 1.f + mod[4 * 576 + k], sk = mod[3 * 576 + k];
#pragma unroll
        for (int e = 0; e < 4; ++e) {
          const float lo = bf2f(v[i][e] & 0xffffu), hi = bfhi2f(v[i][e]);
          c1[2 * e] = fmaf(wk, lo, c1[2 * e]);
          c1[2 * e + 1] = fmaf(wk, hi, c1[2 * e + 1]);
          c2[2 * e] = fmaf(sk, lo, c2[2 * e]);
          c2[2 * e + 1] = fmaf(sk, hi, c2[2 * e + 1]);
        }
      }
#pragma unroll
      for (int e = 0; e < 8; ++e) {
        red[(rg * 144 + cc * 8 + e) * 2] = c1[e];
        red[(rg * 144 + cc * 8 + e) * 2 + 1] = c2[e];
      }
    }
    __syncthreads();
    if (tid < 288) {
      const int col = tid >> 1, which = tid & 1;
      float a = 0.f;
      for (int r = 0; r < 24; ++r) a += red[(r * 144 + col) * 2 + which];
      cv[(mtx * 2 + which) * F_ + col0 + col] = a;
    }
    return;
  }
  const int bm = blockIdx.x & 15, bn = blockIdx.x >> 4;
  float* lnw = (float*)(smem + K2_NSTG * K2_STG);
  for (int i = tid; i < 576; i += 512) {
    lnw[i] = 1.f + mod[576 + i];
    lnw[576 + i] = mod[i];
  }
  const int row0 = bm * 128 + (w & 3) * 32;
  float mu[2], rs[2];
#pragma unroll
  for (int mt = 0; mt < 2; ++mt) {
    const int r = row0 + mt * 16 + (lane & 15);
    mu[mt] = st1[r * 2];
    rs[mt] = st1[r * 2 + 1];
  }
  __syncthreads();
  f32x4 acc[2][9];
#pragma unroll
  for (int i = 0; i < 2; ++i)
#pragma unroll
    for (int j = 0; j < 9; ++j) acc[i][j] = (f32x4){0.f, 0.f, 0.f, 0.f};
  const int which = bn >> 2;
  const int mo = w >> 2;
  const int rw = row0 + mo * 16;
  const u16* bp = wqkv + bn * 144;
  f32x4 res[1][9];
  if (which == 2) {
    gemm_core2<128, 1, 2, 8, K2_NSTG, true, true, 2>(x + (size_t)bm * 128 * D_, D_, bp, bp, NQKV, 9, smem, lnw, 0, mu, rs, acc);
    pair_reduce<4, 2>(acc, res, smem, w, lane);
    const int g = lane >> 4;
#pragma unroll
    for (int t = 0; t < 9; ++t) {
      const int n = (bn & 3) * 144 + t * 16 + (lane & 15);
      const f32x4 v = res[0][t];
      const int pos = 16 * mo + 8 * (g & 1) + 4 * (g >> 1);
      *(u32x2*)(Vt + (size_t)n * S_ + row0 + pos) = (u32x2){pack2(v[0], v[1]), pack2(v[2], v[3])};
    }
    return;
  }
  gemm_core2<128, 1, 2, 8, K2_NSTG, true, false, 2>(x + (size_t)bm * 128 * D_, D_, bp, bp, NQKV, 9, smem, lnw, 0, mu, rs, acc);
  pair_reduce<4, 2>(acc, res, smem, w, lane);
  const float sc = which == 0 ? 0.23570226039551584f * 1.4426950408889634f : 1.f;
  char* tile = smem + 8 * 9 * 64 * 16;
#pragma unroll
  for (int t = 0; t < 9; ++t) {
    const f32x4 v = res[0][t];
    *(u32x2*)(tile + (rw - bm * 128 + (lane & 15)) * 296 + (t * 16 + 4 * (lane >> 4)) * 2) =
        (u32x2){pack2(v[0] * sc, v[1] * sc), pack2(v[2] * sc, v[3] * sc)};
  }
  __syncthreads();
  u16* dm = which == 0 ? Qm : Km;
  u16* dt = which == 0 ? Qt : Kt;
#pragma unroll
  for (int j = 0; j < 2; ++j) {
    const int i = tid + 512 * j, r = i & 127, hl = i >> 7;
    const unsigned* src = (const unsigned*)(tile + r * 296 + hl * 36);
    u32x4 a = {src[0], src[1], src[2], src[3]}, b = {src[4], src[5], src[6], src[7]};
    const unsigned tl = src[8];
    const size_t hr = (size_t)((bn & 3) * 8 + hl) * S_ + bm * 128 + r;
    *(u32x4*)(dm + hr * 16) = a;
    *(u32x4*)(dm + hr * 16 + 8) = b;
    *(unsigned*)(dt + hr * 2) = tl;
  }
}

constexpr int VROW = 4096 + 16;
constexpr int A_KM = 0, A_KT = 65536, A_VT = 65536 + 8192, A_END = A_VT + 20 * VROW;
constexpr int A_SMEM = A_END + 1024;
constexpr float ATT_THR = 8.f;
__global__ __launch_bounds__(1024) void k_attn(const u16* __restrict__ Qm, const u16* __restrict__ Qt,
                                               const u16* __restrict__ Km, const u16* __restrict__ Kt,
                                               const u16* __restrict__ Vt, u16* __restrict__ o,
                                               const u16* __restrict__ pf0, const u16* __restrict__ pf1,
                                               const u16* __restrict__ pf2, const u16* __restrict__ pf3) {
  extern __shared__ __attribute__((aligned(16))) char smem[];
  const int tid = threadIdx.x, lane = tid & 63;
  const int w = __builtin_amdgcn_readfirstlane(tid >> 6);
  const int bid = blockIdx.x;
  const int h = (bid & 7) * 4 + ((bid >> 3) & 3), qc = bid >> 5;
  const u16* kmg = Km + (size_t)h * S_ * 16;
  const u16* ktg = Kt + (size_t)h * S_ * 2;
  const u16* vtg = Vt + (size_t)h * DH_ * S_;
  const int r32 = lane & 31, hh = lane >> 5;
  const int qrow = qc * 256 + (w & 7) * 32 + r32;
  u32x4 qraw;
  unsigned qtw;
  {
    const u16* qp = Qm + ((size_t)h * S_ + qrow) * 16 + hh * 8;
    const u16* qtp = Qt + ((size_t)h * S_ + qrow) * 2;
    asm volatile("global_load_dwordx4 %0, %1, off" : "=&v"(qraw) : "v"(qp) : "memory");
    asm volatile("global_load_dword %0, %1, off" : "=&v"(qtw) : "v"(qtp) : "memory");
  }
#pragma unroll
  for (int c = 0; c < 4; ++c) {
    {
      const int hf = w >> 3, k0 = hf * 1024 + c * 256 + (w & 7) * 32;
      __builtin_amdgcn_global_load_lds((const void*)(kmg + k0 * 16 + lane * 8),
                                       (LDS_AS void*)(smem + A_KM + c * 16384 + w * 1024), 16, 0, 0);
    }
    {
      const int d = w;
      __builtin_amdgcn_global_load_lds((const void*)(vtg + d * S_ + hh * 1024 + c * 256 + r32 * 8),
                                       (LDS_AS void*)(smem + A_VT + d * VROW + c * 1024), 16, 0, 0);
    }
    if (w < 2) {
      const int d = 16 + w;
      __builtin_amdgcn_global_load_lds((const void*)(vtg + d * S_ + hh * 1024 + c * 256 + r32 * 8),
                                       (LDS_AS void*)(smem + A_VT + d * VROW + c * 1024), 16, 0, 0);
    } else if (w < 4) {
      const int hf = w - 2;
      __builtin_amdgcn_global_load_lds((const void*)(ktg + (hf * 1024 + c * 256) * 2 + lane * 8),
                                       (LDS_AS void*)(smem + A_KT + c * 2048 + hf * 1024), 16, 0, 0);
    }
  }
  if (tid < 512) {
    const int rr = 18 + (tid >> 8), cidx = tid & 255;
    const unsigned val = rr == 18 ? 0x3f803f80u : 0u;
    *(u32x4*)(smem + A_VT + rr * VROW + cidx * 16) = (u32x4){val, val, val, val};
  }
  const int khalf = w >> 3;

  const char* kmp = smem + A_KM + khalf * 8192 + r32 * 32 + hh * 16;
  const char* ktp = hh == 0 ? (smem + A_KT + khalf * 1024 + r32 * 4) : (smem + A_VT + 19 * VROW);
  const int ktc = hh == 0 ? 2048 : 0, ktt = hh == 0 ? 128 : 0;
  const int vrow = r32 < 19 ? r32 : 19;
  const char* vp = smem + A_VT + vrow * VROW + khalf * 512 + hh * 16;

  bf16x8 qf0, qf1;
  f32x16 oacc, negm;
#pragma unroll
  for (int i = 0; i < 16; ++i) { negm[i] = 0.f; oacc[i] = 0.f; }
  float m = 0.f;
  auto qk = [&](int tt) -> f32x16 {
    const int c = tt >> 3, t8 = tt & 7;
    bf16x8 kf0 = *(const bf16x8*)(kmp + c * 16384 + t8 * 1024);
    unsigned kt = *(const unsigned*)(ktp + c * ktc + t8 * ktt);
    bf16x8 kf1 = __builtin_bit_cast(bf16x8, (u32x4){kt, 0u, 0u, 0u});
    f32x16 s = __builtin_amdgcn_mfma_f32_32x32x16_bf16(kf0, qf0, negm, 0, 0, 0);
    return __builtin_amdgcn_mfma_f32_32x32x16_bf16(kf1, qf1, s, 0, 0, 0);
  };
  auto pv = [&](int tt, const f32x16& s) {
    const int c = tt >> 3, t8 = tt & 7;
    u32x4 p0, p1;
#pragma unroll
    for (int e = 0; e < 4; ++e) {
      p0[e] = pack2(__builtin_amdgcn_exp2f(s[2 * e]), __builtin_amdgcn_exp2f(s[2 * e + 1]));
      p1[e] = pack2(__builtin_amdgcn_exp2f(s[8 + 2 * e]), __builtin_amdgcn_exp2f(s[8 + 2 * e + 1]));
    }
    bf16x8 vf0 = *(const bf16x8*)(vp + c * 1024 + t8 * 64);
    bf16x8 vf1 = *(const bf16x8*)(vp + c * 1024 + t8 * 64 + 32);
    oacc = __builtin_amdgcn_mfma_f32_32x32x16_bf16(vf0, __builtin_bit_cast(bf16x8, p0), oacc, 0, 0, 0);
    oacc = __builtin_amdgcn_mfma_f32_32x32x16_bf16(vf1, __builtin_bit_cast(bf16x8, p1), oacc, 0, 0, 0);
  };
  auto fixmax = [&](f32x16& sc, f32x16& sn, bool first, bool has_next) {
    float tm = fmaxf(fmaxf(sc[0], sc[1]), sc[2]);
#pragma unroll
    for (int i = 3; i < 15; i += 2) tm = fmaxf(fmaxf(tm, sc[i]), sc[i + 1]);
    tm = fmaxf(tm, sc[15]);
    if (first || __any(tm > ATT_THR)) {
      float d = fmaxf(tm, __shfl_xor(tm, 32));
      if (!first) d = fmaxf(d, 0.f);
      m += d;
      const float al = __builtin_amdgcn_exp2f(-d);
#pragma unroll
      for (int i = 0; i < 16; ++i) {
        negm[i] = -m;
        sc[i] -= d;
        if (has_next) sn[i] -= d;
        if (!first) oacc[i] *= al;
      }
    }
  };
  const int nper = (w < 4) ? 3 : 2;
  f32x16 sA, sB;
#pragma unroll
  for (int c = 0; c < 4; ++c) {
    if (c < 3) {
      if (nper == 3) {
        if (c == 0) wait_vm<9>(); else if (c == 1) wait_vm<6>(); else wait_vm<3>();
      } else {
        if (c == 0) wait_vm<6>(); else if (c == 1) wait_vm<4>(); else wait_vm<2>();
      }
    } else {
      if (bid * 16 + w < 1088) wait_vm<7>(); else wait_vm<4>();
    }
    asm volatile("s_waitcnt lgkmcnt(0)" ::: "memory");
    __builtin_amdgcn_s_barrier();
    asm volatile("" ::: "memory");
    if (c == 0) {
      asm volatile("" : "+v"(qraw), "+v"(qtw));
      qf0 = __builtin_bit_cast(bf16x8, qraw);
      qf1 = __builtin_bit_cast(bf16x8, (u32x4){hh == 0 ? qtw : 0u, 0u, 0u, 0u});
    }
    if (c == 2) {
    {
      const int gw = bid * 16 + w;
#pragma unroll
      for (int i = 0; i < 4; ++i) {
        const u16* src;
        if (i == 0) src = pf0 + ((size_t)(gw % 648) * 512 + lane * 8);
        else if (i == 1) src = pf1 + ((size_t)gw * 512 + lane * 8);
        else if (i == 2) src = pf2 + ((size_t)gw * 512 + lane * 8);
        else src = pf3 + ((size_t)gw * 512 + lane * 8);
        __builtin_amdgcn_global_load_lds((const void*)src, (LDS_AS void*)(smem + A_END), 16, 0, 0);
      }
      if (gw < 1088) {
#pragma unroll
        for (int i = 1; i < 4; ++i) {
          const u16* src = (i == 1 ? pf1 : (i == 2 ? pf2 : pf3)) + ((size_t)(4096 + gw) * 512 + lane * 8);
          __builtin_amdgcn_global_load_lds((const void*)src, (LDS_AS void*)(smem + A_END), 16, 0, 0);
        }
      }
    }
    }
    sA = qk(8 * c);
    if (c == 0) fixmax(sA, sB, true, false);
#pragma unroll 1
    for (int t = 8 * c; t < 8 * c + 8; t += 2) {
      sB = qk(t + 1);
      pv(t, sA);
      if (t + 2 < 8 * c + 8) sA = qk(t + 2);
      pv(t + 1, sB);
    }
  }
  {
    bool bad = false;
#pragma unroll
    for (int i = 0; i < 11; ++i) bad |= !(fabsf(oacc[i]) < 1.0e37f);
    if (__any(bad)) {
#pragma unroll
      for (int i = 0; i < 16; ++i) { negm[i] = 0.f; oacc[i] = 0.f; }
      m = 0.f;
      sA = qk(0);
#pragma unroll 1
      for (int t = 0; t < 32; t += 2) {
        sB = qk(t + 1);
        fixmax(sA, sB, t == 0, true);
        pv(t, sA);
        const bool nx = (t + 2 < 32);
        if (nx) sA = qk(t + 2);
        fixmax(sB, sA, false, nx);
        pv(t + 1, sB);
      }
    }
  }
  __syncthreads();
  float* cb = (float*)(smem + A_KM) + (w & 7) * 64 * 17;
  if (khalf == 1) {
#pragma unroll
    for (int i = 0; i < 11; ++i) cb[i * 64 + lane] = oacc[i];
    cb[11 * 64 + lane] = m;
  }
  __syncthreads();
  if (khalf == 1) return;
  {
    const float m2 = cb[11 * 64 + lane];
    const float mm = fmaxf(m, m2);
    const float a1 = __builtin_amdgcn_exp2f(m - mm), a2 = __builtin_amdgcn_exp2f(m2 - mm);
#pragma unroll
    for (int i = 0; i < 11; ++i) oacc[i] = oacc[i] * a1 + cb[i * 64 + lane] * a2;
  }
  const float l = __shfl(oacc[10], r32);
  const float inv = 1.f / l;
  u16* op = o + (size_t)qrow * D_ + h * DH_;
  {
    u32x2 a = {pack2(oacc[0] * inv, oacc[1] * inv), pack2(oacc[2] * inv, oacc[3] * inv)};
    u32x2 b = {pack2(oacc[4] * inv, oacc[5] * inv), pack2(oacc[6] * inv, oacc[7] * inv)};
    unsigned* p32 = (unsigned*)(op + hh * 4);
    p32[0] = a[0]; p32[1] = a[1];
    p32[4] = b[0]; p32[5] = b[1];
    if (hh == 0) *(unsigned*)(op + 16) = pack2(oacc[8] * inv, oacc[9] * inv);
  }
}

constexpr int K4_NSTG = 5;
constexpr int K4_STG = 2 * 9216 + 64 * 128;
constexpr int K4_SMEM = K4_NSTG * K4_STG;
__global__ __launch_bounds__(256) void k_wo(const u16* __restrict__ o, const u16* __restrict__ wo,
                                            const u16* __restrict__ x, const float* __restrict__ mod,
                                            u16* __restrict__ x1, u16* __restrict__ x1s, float* __restrict__ st2) {
  extern __shared__ __attribute__((aligned(16))) char smem[];
  const int tid = threadIdx.x, lane = tid & 63, w = tid >> 6;
  const int bm = blockIdx.x & 31, bn = blockIdx.x >> 5;
  float mu[2] = {0.f, 0.f}, rs[2] = {0.f, 0.f};
  f32x4 acc[2][9];
#pragma unroll
  for (int i = 0; i < 2; ++i)
#pragma unroll
    for (int j = 0; j < 9; ++j) acc[i][j] = (f32x4){0.f, 0.f, 0.f, 0.f};
  const u16* bp = wo + bn * 144;
  gemm_core2<64, 1, 2, 4, K4_NSTG, false, false, 2>(o + (size_t)bm * 64 * D_, D_, bp, bp, D_, 9, smem, nullptr, 0, mu, rs, acc);
  f32x4 res[1][9];
  pair_reduce<2, 2>(acc, res, smem, w, lane);
  const int r = bm * 64 + (w & 1) * 32 + (w >> 1) * 16 + (lane & 15);
  const float piv = bf2f(x[(size_t)r * D_]);
  float s1 = 0.f, s2 = 0.f;
#pragma unroll
  for (int t = 0; t < 9; ++t) {
    const f32x4 v = res[0][t];
    const int n = bn * 144 + t * 16 + 4 * (lane >> 4);
    const u32x2 xv = *(const u32x2*)(x + (size_t)r * D_ + n);
    const f32x4 gv = *(const f32x4*)(mod + 2 * 576 + n);
    float y[4];
    y[0] = rbf(bf2f(xv[0] & 0xffffu) + rbf(gv[0] * rbf(v[0])));
    y[1] = rbf(bfhi2f(xv[0]) + rbf(gv[1] * rbf(v[1])));
    y[2] = rbf(bf2f(xv[1] & 0xffffu) + rbf(gv[2] * rbf(v[2])));
    y[3] = rbf(bfhi2f(xv[1]) + rbf(gv[3] * rbf(v[3])));
#pragma unroll
    for (int j = 0; j < 4; ++j) { const float d = y[j] - piv; s1 += d; s2 += d * d; }
    const size_t fo = ((size_t)((r >> 4) * 18 + (n >> 5)) * 64 + ((n >> 3) & 3) * 16 + (r & 15)) * 8 + (n & 7);
    const f32x4 cbv = *(const f32x4*)(mod + 4 * 576 + n);
    *(u32x2*)(x1 + fo) = (u32x2){pack2(y[0], y[1]), pack2(y[2], y[3])};
    *(u32x2*)(x1s + fo) = (u32x2){pack2(y[0] * (1.f + cbv[0]), y[1] * (1.f + cbv[1])), pack2(y[2] * (1.f + cbv[2]), y[3] * (1.f + cbv[3]))};
  }
  s1 += __shfl_xor(s1, 16); s1 += __shfl_xor(s1, 32);
  s2 += __shfl_xor(s2, 16); s2 += __shfl_xor(s2, 32);
  if (lane < 16) *(f2_t*)(st2 + (size_t)r * 8 + bn * 2) = (f2_t){s1, s2};
}

constexpr int K5_NSTG = 4;
constexpr int K5_STG = 2 * 9216 + 16 * 1024;
constexpr int K5_SMEM = K5_NSTG * K5_STG + 576 * 4;
__global__ __launch_bounds__(512) void k_gateup(const u16* __restrict__ h2, const u16* __restrict__ wg,
                                                const u16* __restrict__ wu, u16* __restrict__ hh,
                                                const u16* __restrict__ xin, const float* __restrict__ st2,
                                                const float* __restrict__ cv) {
  extern __shared__ __attribute__((aligned(16))) char smem[];
  const int tid = threadIdx.x, lane = tid & 63, w = tid >> 6;
  const int bn = (blockIdx.x & 7) * 4 + ((blockIdx.x >> 3) & 3), bm = blockIdx.x >> 5;
  const int row0 = bm * 256 + (w & 3) * 64;
  float* cvl = (float*)(smem + K5_NSTG * K5_STG);
  for (int i = tid; i < 576; i += 512) cvl[i] = cv[(i / 144) * F_ + bn * 144 + (i % 144)];
  float mu[4], rs[4];
#pragma unroll
  for (int mt = 0; mt < 4; ++mt) {
    const int r = row0 + mt * 16 + (lane & 15);
    const f32x4 a = *(const f32x4*)(st2 + (size_t)r * 8), b = *(const f32x4*)(st2 + (size_t)r * 8 + 4);
    const float s1 = (a[0] + a[2]) + (b[0] + b[2]), s2 = (a[1] + a[3]) + (b[1] + b[3]);
    const float dm = s1 * (1.f / 576.f);
    const float var = fmaxf(s2 * (1.f / 576.f) - dm * dm, 0.f);
    rs[mt] = 1.f / sqrtf(var + 1e-5f);
    mu[mt] = -(bf2f(xin[(size_t)r * D_]) + dm) * rs[mt];
  }
  __syncthreads();
  f32x4 acc[4][9];
#pragma unroll
  for (int i = 0; i < 4; ++i)
#pragma unroll
    for (int j = 0; j < 9; ++j) acc[i][j] = (f32x4){0.f, 0.f, 0.f, 0.f};
  gemm_core2<256, 2, 1, 8, K5_NSTG, false, false, 4, true>(h2 + (size_t)bm * 16 * 18 * 512, 18, wg + bn * 144, wu + bn * 144, F_,
                                                           18, smem, nullptr, 0, mu, rs, acc);
  {
    const float* c1p = cvl + (w >= 4 ? 288 : 0) + 4 * (lane >> 4);
#pragma unroll
    for (int mt = 0; mt < 4; ++mt) {
#pragma unroll
      for (int t = 0; t < 9; ++t) {
        const f32x4 c1 = *(const f32x4*)(c1p + t * 16), c2 = *(const f32x4*)(c1p + 144 + t * 16);
#pragma unroll
        for (int j = 0; j < 4; ++j) acc[mt][t][j] = fmaf(acc[mt][t][j], rs[mt], fmaf(mu[mt], c1[j], c2[j]));
      }
    }
  }
  __syncthreads();
  unsigned* ex = (unsigned*)smem + (w & 3) * 72 * 64 + lane;
  if (w >= 4) {
#pragma unroll
    for (int mt = 0; mt < 4; ++mt)
#pragma unroll
      for (int t = 0; t < 9; ++t) {
        ex[((mt * 9 + t) * 2) * 64] = pack2(acc[mt][t][0], acc[mt][t][1]);
        ex[((mt * 9 + t) * 2 + 1) * 64] = pack2(acc[mt][t][2], acc[mt][t][3]);
      }
  }
  __syncthreads();
  if (w >= 4) return;
#pragma unroll
  for (int mt = 0; mt < 4; ++mt) {
    const int r = row0 + mt * 16 + (lane & 15);
#pragma unroll
    for (int t = 0; t < 9; ++t) {
      const int n = bn * 144 + t * 16 + 4 * (lane >> 4);
      const unsigned u0 = ex[((mt * 9 + t) * 2) * 64], u1 = ex[((mt * 9 + t) * 2 + 1) * 64];
      const float y0 = rbf(acc[mt][t][0]) * bf2f(u0 & 0xffffu), y1 = rbf(acc[mt][t][1]) * bfhi2f(u0);
      const float y2 = rbf(acc[mt][t][2]) * bf2f(u1 & 0xffffu), y3 = rbf(acc[mt][t][3]) * bfhi2f(u1);
      ST_OUT((u32x2*)(hh + ((size_t)((r >> 4) * 144 + (n >> 5)) * 64 + ((n >> 3) & 3) * 16 + (r & 15)) * 8 + (n & 7)),
             ((u32x2){pack2(y0, y1), pack2(y2, y3)}));
    }
  }
}

constexpr int K6_NSTG = 4;
constexpr int K6_STG = 2 * 9216 + 8 * 2 * 1024;
constexpr int K6_SMEM = K6_NSTG * K6_STG;
__global__ __launch_bounds__(512) void k_down(const u16* __restrict__ hh, const u16* __restrict__ wd,
                                              float* __restrict__ part) {
  extern __shared__ __attribute__((aligned(16))) char smem[];
  const int tid = threadIdx.x, lane = tid & 63, w = tid >> 6;
  const int xcd = blockIdx.x & 7, idx = blockIdx.x >> 3;
  const int sk = xcd >> 1, bn = idx & 3, bm = (xcd & 1) * 8 + (idx >> 2);
  float mu[2] = {0.f, 0.f}, rs[2] = {0.f, 0.f};
  f32x4 acc[2][9];
#pragma unroll
  for (int i = 0; i < 2; ++i)
#pragma unroll
    for (int j = 0; j < 9; ++j) acc[i][j] = (f32x4){0.f, 0.f, 0.f, 0.f};
  const u16* bp = wd + (size_t)sk * 1152 * D_ + bn * 144;
  gemm_core2<128, 1, 2, 8, K6_NSTG, false, false, 2, true>(hh + ((size_t)bm * 8 * 144 + sk * 36) * 512, 144, bp, bp, D_, 18, smem,
                                                           nullptr, 0, mu, rs, acc);
  f32x4 res[1][9];
  pair_reduce<4, 2>(acc, res, smem, w, lane);
  float* pp = part + (size_t)sk * S_ * D_;
  const int r = bm * 128 + (w & 3) * 32 + (w >> 2) * 16 + (lane & 15);
#pragma unroll
  for (int t = 0; t < 9; ++t) {
    const int n = bn * 144 + t * 16 + 4 * (lane >> 4);
    ST_OUT((f32x4*)(pp + (size_t)r * D_ + n), res[0][t]);
  }
}

__global__ __launch_bounds__(256) void k_final(const float* __restrict__ part, const u16* __restrict__ x1,
                                               const float* __restrict__ mod, u16* __restrict__ out) {
  const int i = blockIdx.x * 256 + threadIdx.x;
  const int e = i * 8;
  const int n = e % D_;
  f32x4 a0 = *(const f32x4*)(part + e), a1 = *(const f32x4*)(part + e + 4);
#pragma unroll
  for (int s = 1; s < 4; ++s) {
    a0 += *(const f32x4*)(part + (size_t)s * S_ * D_ + e);
    a1 += *(const f32x4*)(part + (size_t)s * S_ * D_ + e + 4);
  }
  const int row = e / D_, c = n >> 3;
  const u32x4 xv = *(const u32x4*)(x1 + ((size_t)((row >> 4) * 18 + (c >> 2)) * 64 + (c & 3) * 16 + (row & 15)) * 8);
  const f32x4 g0 = *(const f32x4*)(mod + 5 * 576 + n), g1 = *(const f32x4*)(mod + 5 * 576 + n + 4);
  u32x4 r;
  r[0] = pack2(bf2f(xv[0] & 0xffffu) + rbf(g0[0] * rbf(a0[0])), bfhi2f(xv[0]) + rbf(g0[1] * rbf(a0[1])));
  r[1] = pack2(bf2f(xv[1] & 0xffffu) + rbf(g0[2] * rbf(a0[2])), bfhi2f(xv[1]) + rbf(g0[3] * rbf(a0[3])));
  r[2] = pack2(bf2f(xv[2] & 0xffffu) + rbf(g1[0] * rbf(a1[0])), bfhi2f(xv[2]) + rbf(g1[1] * rbf(a1[1])));
  r[3] = pack2(bf2f(xv[3] & 0xffffu) + rbf(g1[2] * rbf(a1[2])), bfhi2f(xv[3]) + rbf(g1[3] * rbf(a1[3])));
  *(u32x4*)(out + e) = r;
}

extern "C" void kernel_launch(void* const* d_in, const int* in_sizes, int n_in, void* d_out, int out_size,
                              void* d_ws, size_t ws_size, hipStream_t stream) {
  const u16* x = (const u16*)d_in[0];
  const u16* wqkv = (const u16*)d_in[1];
  const u16* wo = (const u16*)d_in[2];
  const u16* wg = (const u16*)d_in[4];
  const u16* wu = (const u16*)d_in[5];
  const u16* wd = (const u16*)d_in[6];
  const u16* c = (const u16*)d_in[7];
  const u16* wmod = (const u16*)d_in[8];
  char* ws = (char*)d_ws;
  float* mod = (float*)(ws + WS_MOD);
  float* st1 = (float*)(ws + WS_ST1);
  u16* Qm = (u16*)(ws + WS_QM);
  u16* Qt = (u16*)(ws + WS_QT);
  u16* Km = (u16*)(ws + WS_KM);
  u16* Kt = (u16*)(ws + WS_KT);
  u16* Vt = (u16*)(ws + WS_VT);
  u16* o = (u16*)(ws + WS_O);
  u16* x1 = (u16*)(ws + WS_X1);
  float* st2 = (float*)(ws + WS_ST2);
  u16* hh = (u16*)(ws + WS_HH);
  u16* x1s = (u16*)(ws + WS_QM);
  float* cv = (float*)(ws + WS_CV);
  float* part = (float*)(ws + WS_PART);

  (void)hipFuncSetAttribute((const void*)k_qkv, hipFuncAttributeMaxDynamicSharedMemorySize, K2_SMEM);
  (void)hipFuncSetAttribute((const void*)k_attn, hipFuncAttributeMaxDynamicSharedMemorySize, A_SMEM);
  (void)hipFuncSetAttribute((const void*)k_wo, hipFuncAttributeMaxDynamicSharedMemorySize, K4_SMEM);
  (void)hipFuncSetAttribute((const void*)k_gateup, hipFuncAttributeMaxDynamicSharedMemorySize, K5_SMEM);
  (void)hipFuncSetAttribute((const void*)k_down, hipFuncAttributeMaxDynamicSharedMemorySize, K6_SMEM);

  k_prep<<<108 + 64, 256, 0, stream>>>(c, wmod, x, mod, st1);
  k_qkv<<<16 * 12 + 64, 512, K2_SMEM, stream>>>(x, wqkv, mod, st1, Qm, Qt, Km, Kt, Vt, wg, wu, cv);
  k_attn<<<256, 1024, A_SMEM, stream>>>(Qm, Qt, Km, Kt, Vt, o, wo, wg, wu, wd);
  k_wo<<<32 * 4, 256, K4_SMEM, stream>>>(o, wo, x, mod, x1, x1s, st2);
  k_gateup<<<8 * 32, 512, K5_SMEM, stream>>>(x1s, wg, wu, hh, x, st2, cv);
  k_down<<<16 * 4 * 4, 512, K6_SMEM, stream>>>(hh, wd, part);
  k_final<<<S_ * D_ / 8 / 256, 256, 0, stream>>>(part, x1, mod, (u16*)d_out);
}
```

```cpp
#include <hip/hip_runtime.h>
#include <hip/hip_bf16.h>
#include <stdint.h>

typedef short bf16x8 __attribute__((ext_vector_type(8)));
typedef short s16x4 __attribute__((ext_vector_type(4)));
typedef float f32x4 __attribute__((ext_vector_type(4)));
typedef float f32x16 __attribute__((ext_vector_type(16)));
typedef unsigned u32x4 __attribute__((ext_vector_type(4)));
typedef unsigned u32x2 __attribute__((ext_vector_type(2)));
typedef unsigned short u16;
typedef __bf16 bf2_t __attribute__((ext_vector_type(2)));
typedef float f2_t __attribute__((ext_vector_type(2)));

#define DI __device__ __forceinline__
#define ST_OUT(p, v) (*(p) = (v))
#define LDS_AS __attribute__((address_space(3)))

constexpr int S_ = 2048, D_ = 576, H_ = 32, DH_ = 18, F_ = 4608;
constexpr int NQKV = 3 * D_;

DI float bf2f(unsigned v) { return __uint_as_float(v << 16); }
DI float bfhi2f(unsigned v) { return __uint_as_float(v & 0xffff0000u); }
DI unsigned pack2(float lo, float hi) {
  bf2_t r = __builtin_convertvector((f2_t){lo, hi}, bf2_t);
  return __builtin_bit_cast(unsigned, r);
}
DI float rbf(float x) { return bf2f(pack2(x, 0.f) & 0xffffu); }

constexpr size_t WS_MOD = 0;
constexpr size_t WS_ST1 = 16384;
constexpr size_t WS_QM = 32768;
constexpr size_t WS_QT = WS_QM + (size_t)H_ * S_ * 16 * 2;
constexpr size_t WS_KM = WS_QT + (size_t)H_ * S_ * 2 * 2;
constexpr size_t WS_KT = WS_KM + (size_t)H_ * S_ * 16 * 2;
constexpr size_t WS_VT = WS_KT + (size_t)H_ * S_ * 2 * 2;
constexpr size_t WS_O = WS_VT + (size_t)H_ * DH_ * S_ * 2;
constexpr size_t WS_X1 = WS_O + (size_t)S_ * D_ * 2;
constexpr size_t WS_ST2 = WS_X1 + (size_t)S_ * D_ * 2;
constexpr size_t WS_HH = WS_ST2 + (size_t)S_ * 8 * 4;
constexpr size_t WS_PART = WS_HH + (size_t)S_ * F_ * 2;
constexpr size_t WS_WG = WS_PART + (size_t)4 * S_ * D_ * 4;
constexpr size_t WS_WU = WS_WG + (size_t)D_ * F_ * 2;
constexpr size_t WS_CV = WS_WU + (size_t)D_ * F_ * 2;
constexpr size_t WS_END = WS_CV + (size_t)4 * F_ * 4;

__global__ __launch_bounds__(256) void k_prep(const u16* __restrict__ c, const u16* __restrict__ wmod,
                                              const u16* __restrict__ x, float* __restrict__ mod,
                                              float* __restrict__ st1) {
  const int tid = threadIdx.x;
  if (blockIdx.x < 108) {
    __shared__ float red[64][33];
    const int tx = tid & 3, ty = tid >> 2;
    const int col0 = blockIdx.x * 32 + tx * 8;
    float acc[8];
#pragma unroll
    for (int j = 0; j < 8; ++j) acc[j] = 0.f;
    u32x4 w[9];
#pragma unroll
    for (int i = 0; i < 9; ++i) w[i] = *(const u32x4*)(wmod + (size_t)(ty + 64 * i) * 3456 + col0);
#pragma unroll
    for (int i = 0; i < 9; ++i) {
      const float cv = bf2f(c[ty + 64 * i]);
      const float s = rbf(cv / (1.f + __expf(-cv)));
#pragma unroll
      for (int j = 0; j < 4; ++j) {
        acc[2 * j] += s * bf2f(w[i][j] & 0xffffu);
        acc[2 * j + 1] += s * bfhi2f(w[i][j]);
      }
    }
#pragma unroll
    for (int j = 0; j < 8; ++j) red[ty][tx * 8 + j] = acc[j];
    __syncthreads();
    if (tid < 32) {
      float s = 0.f;
      for (int r = 0; r < 64; ++r) s += red[r][tid];
      mod[blockIdx.x * 32 + tid] = rbf(s);
    }
  } else {
    const int lane = tid & 63, w = tid >> 6;
    const int row = (blockIdx.x - 108) * 32 + w * 8 + (lane >> 3), j = lane & 7;
    const u16* xr = x + (size_t)row * D_;
    float v[72];
#pragma unroll
    for (int i = 0; i < 9; ++i) {
      u32x4 q = *(const u32x4*)(xr + (j + 8 * i) * 8);
#pragma unroll
      for (int e = 0; e < 4; ++e) {
        v[i * 8 + 2 * e] = bf2f(q[e] & 0xffffu);
        v[i * 8 + 2 * e + 1] = bfhi2f(q[e]);
      }
    }
    float s = 0.f;
#pragma unroll
    for (int i = 0; i < 72; ++i) s += v[i];
    s += __shfl_xor(s, 1); s += __shfl_xor(s, 2); s += __shfl_xor(s, 4);
    const float mean = s * (1.f / 576.f);
    float q2 = 0.f;
#pragma unroll
    for (int i = 0; i < 72; ++i) { const float d = v[i] - mean; q2 += d * d; }
    q2 += __shfl_xor(q2, 1); q2 += __shfl_xor(q2, 2); q2 += __shfl_xor(q2, 4);
    if (j == 0) {
      st1[row * 2] = mean;
      st1[row * 2 + 1] = 1.f / sqrtf(q2 * (1.f / 576.f) + 1e-5f);
    }
  }
}

#define WAIT_VM(n) asm volatile("s_waitcnt vmcnt(" #n ")" ::: "memory")

template <int N> DI void wait_vm() { asm volatile("s_waitcnt vmcnt(%0)" ::"n"(N) : "memory"); }

#define ISSUE_DIV 2
struct NoMid { DI void operator()() const {} };
template <int BM, int NB, int NKS, int NW, int NSTG, bool LNA, bool SWAP, int MT = 4, bool AFRAG = false, class MID = NoMid>
DI void gemm_core2(const u16* __restrict__ Ag,
                   int lda, const u16* __restrict__ B0, const u16* __restrict__ B1,
                   int ldb, int nstages, char* smem, const float* lnw, int kbeg, const float (&mu)[MT],
                   const float (&rs)[MT], f32x4 (&acc)[MT][9], MID mid = MID()) {
  constexpr int RG = BM / (16 * MT);
  constexpr int NWORK = NB * NKS * RG / NW;
  constexpr int BBYTES = NB * NKS * 9216;
  constexpr int STG = BBYTES + (BM / 16) * NKS * 1024;
  constexpr int APW = (BM / 16) * NKS / NW;
  constexpr int BPW = NB * NKS * 576 / NW;
  constexpr int NBI = (BPW + 63) / 64;
  constexpr int BREM = BPW - (NBI - 1) * 64;
  constexpr int PER = APW + NBI;
  const int tid = threadIdx.x, lane = tid & 63;
  const int w = __builtin_amdgcn_readfirstlane(tid >> 6);
  const int rg = w % RG, wj = w / RG;
  const u16* Bw = ((BPW * w) / (576 * NKS) == 0) ? B0 : B1;
  unsigned bsrc[NBI];
#pragma unroll
  for (int i = 0; i < NBI; ++i) {
    const int c = BPW * w + 64 * i + (lane < BREM || i < NBI - 1 ? lane : 0);
    const int st = c / 576, cin = c - st * 576;
    const int ks = st % NKS;
    const int pos = cin / 18, cc = cin - pos * 18;
    const int r = (pos & 16) | ((pos & 1) << 3) | ((pos >> 1) & 7);
    bsrc[i] = (unsigned)((ks * 32 + r) * ldb + cc * 8);
  }
  unsigned asrc[APW];
#pragma unroll
  for (int i = 0; i < APW; ++i) {
    if constexpr (AFRAG) {
      const int blk = w * APW + i, ks = blk / (BM / 16), rt = blk - ks * (BM / 16);
      asrc[i] = (unsigned)(((rt * lda + ks) * 64 + lane) * 8);
    } else {
      const int blk = w * APW + i, kp = blk / (BM / 8), r8 = blk - kp * (BM / 8);
      asrc[i] = (unsigned)((r8 * 8 + (lane >> 3)) * lda + kp * 64 + (((lane & 7) ^ (lane >> 3)) * 8));
    }
  }
  auto issue_one = [&](int s, int i) {
    char* sb = smem + (s % NSTG) * STG;
    if (i < APW) {
      __builtin_amdgcn_global_load_lds((const void*)(Ag + s * NKS * (AFRAG ? 512 : 32) + asrc[i]), (LDS_AS void*)(sb + BBYTES + (w * APW + i) * 1024), 16, 0, 0);
    } else {
      const int j = i - APW;
      const u16* bb = Bw + (size_t)s * NKS * 32 * ldb;
      if (j < NBI - 1 || BREM == 64 || lane < BREM)
        __builtin_amdgcn_global_load_lds((const void*)(bb + bsrc[j]), (LDS_AS void*)(sb + (BPW * w + 64 * j) * 16), 16, 0, 0);
    }
  };
#pragma unroll
  for (int s = 0; s < NSTG - 1; ++s)
    if (s < nstages) {
#pragma unroll
      for (int i = 0; i < PER; ++i) issue_one(s, i);
    }
  mid();
  const int g = lane >> 4, q = (lane & 15) >> 2, p = lane & 3;
  const int brd = (16 * (g >> 1) + 2 * q + (g & 1)) * 288 + 8 * p;
  for (int s = 0; s < nstages; ++s) {
    if (s + NSTG - 2 < nstages) wait_vm<(NSTG - 2) * PER>();
    else wait_vm<0>();
    asm volatile("s_waitcnt lgkmcnt(0)" ::: "memory");
    __builtin_amdgcn_s_barrier();
    asm volatile("" ::: "memory");
    const bool more = (s + NSTG - 1 < nstages);
    char* sb = smem + (s % NSTG) * STG;
#pragma unroll
    for (int i = 0; i < NWORK; ++i) {
      const int u = wj * NWORK + i, b = u / NKS, ks = u - b * NKS;
      bf16x8 af[MT];
#pragma unroll
      for (int mt = 0; mt < MT; ++mt) {
        u32x4 raw;
        if constexpr (AFRAG) raw = *(const u32x4*)(sb + BBYTES + (ks * (BM / 16) + rg * MT + mt) * 1024 + lane * 16);
        else raw = *(const u32x4*)(sb + BBYTES + (ks >> 1) * BM * 128 + ((rg * MT + mt) * 16 + (lane & 15)) * 128 +
                                   (((4 * (ks & 1) + g) ^ (lane & 7)) * 16));
        if constexpr (LNA) {
          const int k = kbeg + (s * NKS + ks) * 32 + 8 * g;
          const f32x4 w0 = *(const f32x4*)(lnw + k), w1 = *(const f32x4*)(lnw + k + 4);
          const f32x4 b0 = *(const f32x4*)(lnw + 576 + k), b1 = *(const f32x4*)(lnw + 576 + k + 4);
          const float nm = -mu[mt] * rs[mt];
          u32x4 o;
#pragma unroll
          for (int e = 0; e < 4; ++e) {
            const float lo = fmaf(bf2f(raw[e] & 0xffffu), rs[mt], nm);
            const float hi = fmaf(bfhi2f(raw[e]), rs[mt], nm);
            const float wl = e < 2 ? w0[2 * e] : w1[2 * e - 4], wh = e < 2 ? w0[2 * e + 1] : w1[2 * e - 3];
            const float bl = e < 2 ? b0[2 * e] : b1[2 * e - 4], bh = e < 2 ? b0[2 * e + 1] : b1[2 * e - 3];
            o[e] = pack2(fmaf(lo, wl, bl), fmaf(hi, wh, bh));
          }
          raw = o;
        }
        af[mt] = __builtin_bit_cast(bf16x8, raw);
      }
      const char* bbase = sb + (b * NKS + ks) * 9216 + brd;
#pragma unroll
      for (int t = 0; t < 9; ++t) {
        s16x4 lo = __builtin_amdgcn_ds_read_tr16_b64_v4i16((LDS_AS s16x4*)(bbase + t * 32));
        s16x4 hi = __builtin_amdgcn_ds_read_tr16_b64_v4i16((LDS_AS s16x4*)(bbase + t * 32 + 2304));
        bf16x8 bf = __builtin_shufflevector(lo, hi, 0, 1, 2, 3, 4, 5, 6, 7);
#pragma unroll
        for (int mt = 0; mt < MT; ++mt) {
          if constexpr (SWAP) acc[mt][t] = __builtin_amdgcn_mfma_f32_16x16x32_bf16(af[mt], bf, acc[mt][t], 0, 0, 0);
          else acc[mt][t] = __builtin_amdgcn_mfma_f32_16x16x32_bf16(bf, af[mt], acc[mt][t], 0, 0, 0);
        }
        constexpr int NIT = (NWORK * 9 + 1) / ISSUE_DIV;
        const int it = i * 9 + t;
        if (more && it < NIT) {
#pragma unroll
          for (int e = it * PER / NIT; e < (it + 1) * PER / NIT; ++e) issue_one(s + NSTG - 1, e);
        }
      }
    }
  }
}

template <int X, int MT>
DI void pair_reduce(f32x4 (&acc)[MT][9], f32x4 (&res)[MT / 2][9], char* smem, int w, int lane) {
  constexpr int HM = MT / 2;
  f32x4* mine = (f32x4*)smem + w * HM * 9 * 64 + lane;
  f32x4* theirs = (f32x4*)smem + (w ^ X) * HM * 9 * 64 + lane;
  const bool hi = __builtin_amdgcn_readfirstlane(w & X) != 0;
  __syncthreads();
#pragma unroll
  for (int mt = 0; mt < HM; ++mt)
#pragma unroll
    for (int t = 0; t < 9; ++t) {
      f32x4 snd, kp;
#pragma unroll
      for (int j = 0; j < 4; ++j) {
        const float lo = acc[mt][t][j], up = acc[HM + mt][t][j];
        snd[j] = hi ? lo : up;
        kp[j] = hi ? up : lo;
      }
      mine[(mt * 9 + t) * 64] = snd;
      res[mt][t] = kp;
    }
  __syncthreads();
#pragma unroll
  for (int mt = 0; mt < HM; ++mt)
#pragma unroll
    for (int t = 0; t < 9; ++t) res[mt][t] += theirs[(mt * 9 + t) * 64];
}

constexpr int K2_NSTG = 4;
constexpr int K2_STG = 2 * 9216 + 128 * 128;
constexpr int K2_SMEM = K2_NSTG * K2_STG + 2 * 576 * 4;
__global__ __launch_bounds__(512) void k_qkv(const u16* __restrict__ x, const u16* __restrict__ wqkv,
                                             const float* __restrict__ mod, const float* __restrict__ st1,
                                             u16* __restrict__ Qm, u16* __restrict__ Qt, u16* __restrict__ Km,
                                             u16* __restrict__ Kt, u16* __restrict__ Vt, const u16* __restrict__ wg,
                                             const u16* __restrict__ wu, float* __restrict__ cv) {
  extern __shared__ __attribute__((aligned(16))) char smem[];
  const int tid = threadIdx.x, lane = tid & 63, w = tid >> 6;
  if (blockIdx.x >= 192) {
    const int j = blockIdx.x - 192, mtx = j >> 5, col0 = (j & 31) * 144;
    const u16* src = (mtx ? wu : wg) + col0;
    float* red = (float*)smem;
    if (tid < 432) {
      const int cc = tid % 18, rg = tid / 18;
      float c1[8], c2[8];
#pragma unroll
      for (int e = 0; e < 8; ++e) { c1[e] = 0.f; c2[e] = 0.f; }
      u32x4 v[24];
#pragma unroll
      for (int i = 0; i < 24; ++i) v[i] = *(const u32x4*)(src + (size_t)(rg + 24 * i) * F_ + cc * 8);
#pragma unroll
      for (int i = 0; i < 24; ++i) {
        const int k = rg + 24 * i;
        const float wk = 1.f + mod[4 * 576 + k], sk = mod[3 * 576 + k];
#pragma unroll
        for (int e = 0; e < 4; ++e) {
          const float lo = bf2f(v[i][e] & 0xffffu), hi = bfhi2f(v[i][e]);
          c1[2 * e] = fmaf(wk, lo, c1[2 * e]);
          c1[2 * e + 1] = fmaf(wk, hi, c1[2 * e + 1]);
          c2[2 * e] = fmaf(sk, lo, c2[2 * e]);
          c2[2 * e + 1] = fmaf(sk, hi, c2[2 * e + 1]);
        }
      }
#pragma unroll
      for (int e = 0; e < 8; ++e) {
        red[(rg * 144 + cc * 8 + e) * 2] = c1[e];
        red[(rg * 144 + cc * 8 + e) * 2 + 1] = c2[e];
      }
    }
    __syncthreads();
    if (tid < 288) {
      const int col = tid >> 1, which = tid & 1;
      float a = 0.f;
      for (int r = 0; r < 24; ++r) a += red[(r * 144 + col) * 2 + which];
      cv[(mtx * 2 + which) * F_ + col0 + col] = a;
    }
    return;
  }
  const int bm = blockIdx.x & 15, bn = blockIdx.x >> 4;
  float* lnw = (float*)(smem + K2_NSTG * K2_STG);
  const int row0 = bm * 128 + (w & 3) * 32;
  float mu[2], rs[2];
  auto mid = [&]() {
    for (int i = tid; i < 576; i += 512) {
      lnw[i] = 1.f + mod[576 + i];
      lnw[576 + i] = mod[i];
    }
#pragma unroll
    for (int mt = 0; mt < 2; ++mt) {
      const int r = row0 + mt * 16 + (lane & 15);
      mu[mt] = st1[r * 2];
      rs[mt] = st1[r * 2 + 1];
    }
    asm volatile("s_waitcnt vmcnt(0) lgkmcnt(0)" ::: "memory");
  };
  f32x4 acc[2][9];
#pragma unroll
  for (int i = 0; i < 2; ++i)
#pragma unroll
    for (int j = 0; j < 9; ++j) acc[i][j] = (f32x4){0.f, 0.f, 0.f, 0.f};
  mid(); __syncthreads();
  const int which = bn >> 2;
  const int mo = w >> 2;
  const int rw = row0 + mo * 16;
  const u16* bp = wqkv + bn * 144;
  f32x4 res[1][9];
  if (which == 2) {
    gemm_core2<128, 1, 2, 8, K2_NSTG, true, true, 2, false>(x + (size_t)bm * 128 * D_, D_, bp, bp, NQKV, 9, smem, lnw, 0, mu, rs, acc);
    pair_reduce<4, 2>(acc, res, smem, w, lane);
    const int g = lane >> 4;
#pragma unroll
    for (int t = 0; t < 9; ++t) {
      const int n = (bn & 3) * 144 + t * 16 + (lane & 15);
      const f32x4 v = res[0][t];
      const int pos = 16 * mo + 8 * (g & 1) + 4 * (g >> 1);
      *(u32x2*)(Vt + (size_t)n * S_ + row0 + pos) = (u32x2){pack2(v[0], v[1]), pack2(v[2], v[3])};
    }
    return;
  }
  gemm_core2<128, 1, 2, 8, K2_NSTG, true, false, 2, false>(x + (size_t)bm * 128 * D_, D_, bp, bp, NQKV, 9, smem, lnw, 0, mu, rs, acc);
  pair_reduce<4, 2>(acc, res, smem, w, lane);
  const float sc = which == 0 ? 0.23570226039551584f * 1.4426950408889634f : 1.f;
  char* tile = smem + 8 * 9 * 64 * 16;
#pragma unroll
  for (int t = 0; t < 9; ++t) {
    const f32x4 v = res[0][t];
    *(u32x2*)(tile + (rw - bm * 128 + (lane & 15)) * 296 + (t * 16 + 4 * (lane >> 4)) * 2) =
        (u32x2){pack2(v[0] * sc, v[1] * sc), pack2(v[2] * sc, v[3] * sc)};
  }
  __syncthreads();
  u16* dm = which == 0 ? Qm : Km;
  u16* dt = which == 0 ? Qt : Kt;
#pragma unroll
  for (int j = 0; j < 2; ++j) {
    const int i = tid + 512 * j, r = i & 127, hl = i >> 7;
    const unsigned* src = (const unsigned*)(tile + r * 296 + hl * 36);
    u32x4 a = {src[0], src[1], src[2], src[3]}, b = {src[4], src[5], src[6], src[7]};
    const unsigned tl = src[8];
    const size_t hr = (size_t)((bn & 3) * 8 + hl) * S_ + bm * 128 + r;
    *(u32x4*)(dm + hr * 16) = a;
    *(u32x4*)(dm + hr * 16 + 8) = b;
    *(unsigned*)(dt + hr * 2) = tl;
  }
}

constexpr int VROW = 4096 + 16;
constexpr int A_KM = 0, A_KT = 65536, A_VT = 65536 + 8192, A_END = A_VT + 20 * VROW;
constexpr int A_SMEM = A_END + 1024;
constexpr float ATT_THR = 8.f;
__global__ __launch_bounds__(1024) void k_attn(const u16* __restrict__ Qm, const u16* __restrict__ Qt,
                                               const u16* __restrict__ Km, const u16* __restrict__ Kt,
                                               const u16* __restrict__ Vt, u16* __restrict__ o,
                                               const u16* __restrict__ pf0, const u16* __restrict__ pf1,
                                               const u16* __restrict__ pf2, const u16* __restrict__ pf3) {
  extern __shared__ __attribute__((aligned(16))) char smem[];
  const int tid = threadIdx.x, lane = tid & 63;
  const int w = __builtin_amdgcn_readfirstlane(tid >> 6);
  const int bid = blockIdx.x;
  const int h = (bid & 7) * 4 + ((bid >> 3) & 3), qc = bid >> 5;
  const u16* kmg = Km + (size_t)h * S_ * 16;
  const u16* ktg = Kt + (size_t)h * S_ * 2;
  const u16* vtg = Vt + (size_t)h * DH_ * S_;
  const int r32 = lane & 31, hh = lane >> 5;
  const int qrow = qc * 256 + (w & 7) * 32 + r32;
  u32x4 qraw;
  unsigned qtw;
  {
    const u16* qp = Qm + ((size_t)h * S_ + qrow) * 16 + hh * 8;
    const u16* qtp = Qt + ((size_t)h * S_ + qrow) * 2;
    asm volatile("global_load_dwordx4 %0, %1, off" : "=&v"(qraw) : "v"(qp) : "memory");
    asm volatile("global_load_dword %0, %1, off" : "=&v"(qtw) : "v"(qtp) : "memory");
  }
#pragma unroll
  for (int c = 0; c < 4; ++c) {
    {
      const int hf = w >> 3, k0 = hf * 1024 + c * 256 + (w & 7) * 32;
      __builtin_amdgcn_global_load_lds((const void*)(kmg + k0 * 16 + lane * 8),
                                       (LDS_AS void*)(smem + A_KM + c * 16384 + w * 1024), 16, 0, 0);
    }
    {
      const int d = w;
      __builtin_amdgcn_global_load_lds((const void*)(vtg + d * S_ + hh * 1024 + c * 256 + r32 * 8),
                                       (LDS_AS void*)(smem + A_VT + d * VROW + c * 1024), 16, 0, 0);
    }
    if (w < 2) {
      const int d = 16 + w;
      __builtin_amdgcn_global_load_lds((const void*)(vtg + d * S_ + hh * 1024 + c * 256 + r32 * 8),
                                       (LDS_AS void*)(smem + A_VT + d * VROW + c * 1024), 16, 0, 0);
    } else if (w < 4) {
      const int hf = w - 2;
      __builtin_amdgcn_global_load_lds((const void*)(ktg + (hf * 1024 + c * 256) * 2 + lane * 8),
                                       (LDS_AS void*)(smem + A_KT + c * 2048 + hf * 1024), 16, 0, 0);
    }
  }
  if (tid < 512) {
    const int rr = 18 + (tid >> 8), cidx = tid & 255;
    const unsigned val = rr == 18 ? 0x3f803f80u : 0u;
    *(u32x4*)(smem + A_VT + rr * VROW + cidx * 16) = (u32x4){val, val, val, val};
  }
  const int khalf = w >> 3;

  const char* kmp = smem + A_KM + khalf * 8192 + r32 * 32 + hh * 16;
  const char* ktp = hh == 0 ? (smem + A_KT + khalf * 1024 + r32 * 4) : (smem + A_VT + 19 * VROW);
  const int ktc = hh == 0 ? 2048 : 0, ktt = hh == 0 ? 128 : 0;
  const int vrow = r32 < 19 ? r32 : 19;
  const char* vp = smem + A_VT + vrow * VROW + khalf * 512 + hh * 16;

  bf16x8 qf0, qf1;
  f32x16 oacc, negm;
#pragma unroll
  for (int i = 0; i < 16; ++i) { negm[i] = 0.f; oacc[i] = 0.f; }
  float m = 0.f;
  auto qk = [&](int tt) -> f32x16 {
    const int c = tt >> 3, t8 = tt & 7;
    bf16x8 kf0 = *(const bf16x8*)(kmp + c * 16384 + t8 * 1024);
    unsigned kt = *(const unsigned*)(ktp + c * ktc + t8 * ktt);
    bf16x8 kf1 = __builtin_bit_cast(bf16x8, (u32x4){kt, 0u, 0u, 0u});
    f32x16 s = __builtin_amdgcn_mfma_f32_32x32x16_bf16(kf0, qf0, negm, 0, 0, 0);
    return __builtin_amdgcn_mfma_f32_32x32x16_bf16(kf1, qf1, s, 0, 0, 0);
  };
  auto pv = [&](int tt, const f32x16& s) {
    const int c = tt >> 3, t8 = tt & 7;
    u32x4 p0, p1;
#pragma unroll
    for (int e = 0; e < 4; ++e) {
      p0[e] = pack2(__builtin_amdgcn_exp2f(s[2 * e]), __builtin_amdgcn_exp2f(s[2 * e + 1]));
      p1[e] = pack2(__builtin_amdgcn_exp2f(s[8 + 2 * e]), __builtin_amdgcn_exp2f(s[8 + 2 * e + 1]));
    }
    bf16x8 vf0 = *(const bf16x8*)(vp + c * 1024 + t8 * 64);
    bf16x8 vf1 = *(const bf16x8*)(vp + c * 1024 + t8 * 64 + 32);
    oacc = __builtin_amdgcn_mfma_f32_32x32x16_bf16(vf0, __builtin_bit_cast(bf16x8, p0), oacc, 0, 0, 0);
    oacc = __builtin_amdgcn_mfma_f32_32x32x16_bf16(vf1, __builtin_bit_cast(bf16x8, p1), oacc, 0, 0, 0);
  };
  auto fixmax = [&](f32x16& sc, f32x16& sn, bool first, bool has_next) {
    float tm = fmaxf(fmaxf(sc[0], sc[1]), sc[2]);
#pragma unroll
    for (int i = 3; i < 15; i += 2) tm = fmaxf(fmaxf(tm, sc[i]), sc[i + 1]);
    tm = fmaxf(tm, sc[15]);
    if (first || __any(tm > ATT_THR)) {
      float d = fmaxf(tm, __shfl_xor(tm, 32));
      if (!first) d = fmaxf(d, 0.f);
      m += d;
      const float al = __builtin_amdgcn_exp2f(-d);
#pragma unroll
      for (int i = 0; i < 16; ++i) {
        negm[i] = -m;
        sc[i] -= d;
        if (has_next) sn[i] -= d;
        if (!first) oacc[i] *= al;
      }
    }
  };
  const int nper = (w < 4) ? 3 : 2;
  f32x16 sA, sB;
#pragma unroll
  for (int c = 0; c < 4; ++c) {
    if (c < 3) {
      if (nper == 3) {
        if (c == 0) wait_vm<9>(); else if (c == 1) wait_vm<6>(); else wait_vm<3>();
      } else {
        if (c == 0) wait_vm<6>(); else if (c == 1) wait_vm<4>(); else wait_vm<2>();
      }
    } else {
      if (bid * 16 + w < 1088) wait_vm<7>(); else wait_vm<4>();
    }
    asm volatile("s_waitcnt lgkmcnt(0)" ::: "memory");
    __builtin_amdgcn_s_barrier();
    asm volatile("" ::: "memory");
    if (c == 0) {
      asm volatile("" : "+v"(qraw), "+v"(qtw));
      qf0 = __builtin_bit_cast(bf16x8, qraw);
      qf1 = __builtin_bit_cast(bf16x8, (u32x4){hh == 0 ? qtw : 0u, 0u, 0u, 0u});
    }
    if (c == 2) {
    {
      const int gw = bid * 16 + w;
#pragma unroll
      for (int i = 0; i < 4; ++i) {
        const u16* src;
        if (i == 0) src = pf0 + ((size_t)(gw % 648) * 512 + lane * 8);
        else if (i == 1) src = pf1 + ((size_t)gw * 512 + lane * 8);
        else if (i == 2) src = pf2 + ((size_t)gw * 512 + lane * 8);
        else src = pf3 + ((size_t)gw * 512 + lane * 8);
        __builtin_amdgcn_global_load_lds((const void*)src, (LDS_AS void*)(smem + A_END), 16, 0, 0);
      }
      if (gw < 1088) {
#pragma unroll
        for (int i = 1; i < 4; ++i) {
          const u16* src = (i == 1 ? pf1 : (i == 2 ? pf2 : pf3)) + ((size_t)(4096 + gw) * 512 + lane * 8);
          __builtin_amdgcn_global_load_lds((const void*)src, (LDS_AS void*)(smem + A_END), 16, 0, 0);
        }
      }
    }
    }
    sA = qk(8 * c);
    if (c == 0) fixmax(sA, sB, true, false);
#pragma unroll
    for (int t = 8 * c; t < 8 * c + 8; t += 2) {
      sB = qk(t + 1);
      pv(t, sA);
      if (t + 2 < 8 * c + 8) sA = qk(t + 2);
      pv(t + 1, sB);
    }
  }
  {
    bool bad = false;
#pragma unroll
    for (int i = 0; i < 11; ++i) bad |= !(fabsf(oacc[i]) < 1.0e37f);
    if (__any(bad)) {
#pragma unroll
      for (int i = 0; i < 16; ++i) { negm[i] = 0.f; oacc[i] = 0.f; }
      m = 0.f;
      sA = qk(0);
#pragma unroll 1
      for (int t = 0; t < 32; t += 2) {
        sB = qk(t + 1);
        fixmax(sA, sB, t == 0, true);
        pv(t, sA);
        const bool nx = (t + 2 < 32);
        if (nx) sA = qk(t + 2);
        fixmax(sB, sA, false, nx);
        pv(t + 1, sB);
      }
    }
  }
  __syncthreads();
  float* cb = (float*)(smem + A_KM) + (w & 7) * 64 * 17;
  if (khalf == 1) {
#pragma unroll
    for (int i = 0; i < 11; ++i) cb[i * 64 + lane] = oacc[i];
    cb[11 * 64 + lane] = m;
  }
  __syncthreads();
  if (khalf == 1) return;
  {
    const float m2 = cb[11 * 64 + lane];
    const float mm = fmaxf(m, m2);
    const float a1 = __builtin_amdgcn_exp2f(m - mm), a2 = __builtin_amdgcn_exp2f(m2 - mm);
#pragma unroll
    for (int i = 0; i < 11; ++i) oacc[i] = oacc[i] * a1 + cb[i * 64 + lane] * a2;
  }
  const float l = __shfl(oacc[10], r32);
  const float inv = 1.f / l;
  u16* op = o + (size_t)qrow * D_ + h * DH_;
  {
    u32x2 a = {pack2(oacc[0] * inv, oacc[1] * inv), pack2(oacc[2] * inv, oacc[3] * inv)};
    u32x2 b = {pack2(oacc[4] * inv, oacc[5] * inv), pack2(oacc[6] * inv, oacc[7] * inv)};
    unsigned* p32 = (unsigned*)(op + hh * 4);
    p32[0] = a[0]; p32[1] = a[1];
    p32[4] = b[0]; p32[5] = b[1];
    if (hh == 0) *(unsigned*)(op + 16) = pack2(oacc[8] * inv, oacc[9] * inv);
  }
}

constexpr int K4_NSTG = 5;
constexpr int K4_STG = 2 * 9216 + 64 * 128;
constexpr int K4_SMEM = K4_NSTG * K4_STG;
__global__ __launch_bounds__(256) void k_wo(const u16* __restrict__ o, const u16* __restrict__ wo,
                                            const u16* __restrict__ x, const float* __restrict__ mod,
                                            u16* __restrict__ x1, u16* __restrict__ x1s, float* __restrict__ st2) {
  extern __shared__ __attribute__((aligned(16))) char smem[];
  const int tid = threadIdx.x, lane = tid & 63, w = tid >> 6;
  const int bm = blockIdx.x & 31, bn = blockIdx.x >> 5;
  float mu[2] = {0.f, 0.f}, rs[2] = {0.f, 0.f};
  f32x4 acc[2][9];
#pragma unroll
  for (int i = 0; i < 2; ++i)
#pragma unroll
    for (int j = 0; j < 9; ++j) acc[i][j] = (f32x4){0.f, 0.f, 0.f, 0.f};
  const u16* bp = wo + bn * 144;
  const int r = bm * 64 + (w & 1) * 32 + (w >> 1) * 16 + (lane & 15);
  const float piv = bf2f(x[(size_t)r * D_]);
  u32x2 xv[9];
  f32x4 gv[9], cbv[9];
#pragma unroll
  for (int t = 0; t < 9; ++t) {
    const int n = bn * 144 + t * 16 + 4 * (lane >> 4);
    xv[t] = *(const u32x2*)(x + (size_t)r * D_ + n);
    gv[t] = *(const f32x4*)(mod + 2 * 576 + n);
    cbv[t] = *(const f32x4*)(mod + 4 * 576 + n);
  }
  asm volatile("" ::: "memory");
  gemm_core2<64, 1, 2, 4, K4_NSTG, false, false, 2>(o + (size_t)bm * 64 * D_, D_, bp, bp, D_, 9, smem, nullptr, 0, mu, rs, acc);
  f32x4 res[1][9];
  pair_reduce<2, 2>(acc, res, smem, w, lane);
  float s1 = 0.f, s2 = 0.f;
#pragma unroll
  for (int t = 0; t < 9; ++t) {
    const f32x4 v = res[0][t];
    const int n = bn * 144 + t * 16 + 4 * (lane >> 4);
    float y[4];
    y[0] = rbf(bf2f(xv[t][0] & 0xffffu) + rbf(gv[t][0] * rbf(v[0])));
    y[1] = rbf(bfhi2f(xv[t][0]) + rbf(gv[t][1] * rbf(v[1])));
    y[2] = rbf(bf2f(xv[t][1] & 0xffffu) + rbf(gv[t][2] * rbf(v[2])));
    y[3] = rbf(bfhi2f(xv[t][1]) + rbf(gv[t][3] * rbf(v[3])));
#pragma unroll
    for (int j = 0; j < 4; ++j) { const float d = y[j] - piv; s1 += d; s2 += d * d; }
    const size_t fo = ((size_t)((r >> 4) * 18 + (n >> 5)) * 64 + ((n >> 3) & 3) * 16 + (r & 15)) * 8 + (n & 7);
    *(u32x2*)(x1 + fo) = (u32x2){pack2(y[0], y[1]), pack2(y[2], y[3])};
    *(u32x2*)(x1s + fo) = (u32x2){pack2(y[0] * (1.f + cbv[t][0]), y[1] * (1.f + cbv[t][1])),
                                  pack2(y[2] * (1.f + cbv[t][2]), y[3] * (1.f + cbv[t][3]))};
  }
  s1 += __shfl_xor(s1, 16); s1 += __shfl_xor(s1, 32);
  s2 += __shfl_xor(s2, 16); s2 += __shfl_xor(s2, 32);
  if (lane < 16) *(f2_t*)(st2 + (size_t)r * 8 + bn * 2) = (f2_t){s1, s2};
}

constexpr int K5_NSTG = 4;
constexpr int K5_STG = 2 * 9216 + 16 * 1024;
constexpr int K5_SMEM = K5_NSTG * K5_STG + 576 * 4;
__global__ __launch_bounds__(512) void k_gateup(const u16* __restrict__ h2, const u16* __restrict__ wg,
                                                const u16* __restrict__ wu, u16* __restrict__ hh,
                                                const u16* __restrict__ xin, const float* __restrict__ st2,
                                                const float* __restrict__ cv) {
  extern __shared__ __attribute__((aligned(16))) char smem[];
  const int tid = threadIdx.x, lane = tid & 63, w = tid >> 6;
  const int bn = (blockIdx.x & 7) * 4 + ((blockIdx.x >> 3) & 3), bm = blockIdx.x >> 5;
  const int row0 = bm * 256 + (w & 3) * 64;
  float* cvl = (float*)(smem + K5_NSTG * K5_STG);
  float mu[4], rs[4];
  auto mid = [&]() {
    for (int i = tid; i < 576; i += 512) cvl[i] = cv[(i / 144) * F_ + bn * 144 + (i % 144)];
#pragma unroll
    for (int mt = 0; mt < 4; ++mt) {
      const int r = row0 + mt * 16 + (lane & 15);
      const f32x4 a = *(const f32x4*)(st2 + (size_t)r * 8), b = *(const f32x4*)(st2 + (size_t)r * 8 + 4);
      const float s1 = (a[0] + a[2]) + (b[0] + b[2]), s2 = (a[1] + a[3]) + (b[1] + b[3]);
      const float dm = s1 * (1.f / 576.f);
      const float var = fmaxf(s2 * (1.f / 576.f) - dm * dm, 0.f);
      rs[mt] = 1.f / sqrtf(var + 1e-5f);
      mu[mt] = -(bf2f(xin[(size_t)r * D_]) + dm) * rs[mt];
    }
    asm volatile("s_waitcnt vmcnt(0) lgkmcnt(0)" ::: "memory");
  };
  f32x4 acc[4][9];
#pragma unroll
  for (int i = 0; i < 4; ++i)
#pragma unroll
    for (int j = 0; j < 9; ++j) acc[i][j] = (f32x4){0.f, 0.f, 0.f, 0.f};
  gemm_core2<256, 2, 1, 8, K5_NSTG, false, false, 4, true>(h2 + (size_t)bm * 16 * 18 * 512, 18, wg + bn * 144, wu + bn * 144, F_,
                                                           18, smem, nullptr, 0, mu, rs, acc, mid);
  {
    const float* c1p = cvl + (w >= 4 ? 288 : 0) + 4 * (lane >> 4);
#pragma unroll
    for (int mt = 0; mt < 4; ++mt) {
#pragma unroll
      for (int t = 0; t < 9; ++t) {
        const f32x4 c1 = *(const f32x4*)(c1p + t * 16), c2 = *(const f32x4*)(c1p + 144 + t * 16);
#pragma unroll
        for (int j = 0; j < 4; ++j) acc[mt][t][j] = fmaf(acc[mt][t][j], rs[mt], fmaf(mu[mt], c1[j], c2[j]));
      }
    }
  }
  __syncthreads();
  unsigned* ex = (unsigned*)smem + (w & 3) * 72 * 64 + lane;
  if (w >= 4) {
#pragma unroll
    for (int mt = 0; mt < 4; ++mt)
#pragma unroll
      for (int t = 0; t < 9; ++t) {
        ex[((mt * 9 + t) * 2) * 64] = pack2(acc[mt][t][0], acc[mt][t][1]);
        ex[((mt * 9 + t) * 2 + 1) * 64] = pack2(acc[mt][t][2], acc[mt][t][3]);
      }
  }
  __syncthreads();
  if (w >= 4) return;
#pragma unroll
  for (int mt = 0; mt < 4; ++mt) {
    const int r = row0 + mt * 16 + (lane & 15);
#pragma unroll
    for (int t = 0; t < 9; ++t) {
      const int n = bn * 144 + t * 16 + 4 * (lane >> 4);
      const unsigned u0 = ex[((mt * 9 + t) * 2) * 64], u1 = ex[((mt * 9 + t) * 2 + 1) * 64];
      const float y0 = rbf(acc[mt][t][0]) * bf2f(u0 & 0xffffu), y1 = rbf(acc[mt][t][1]) * bfhi2f(u0);
      const float y2 = rbf(acc[mt][t][2]) * bf2f(u1 & 0xffffu), y3 = rbf(acc[mt][t][3]) * bfhi2f(u1);
      ST_OUT((u32x2*)(hh + ((size_t)((r >> 4) * 144 + (n >> 5)) * 64 + ((n >> 3) & 3) * 16 + (r & 15)) * 8 + (n & 7)),
             ((u32x2){pack2(y0, y1), pack2(y2, y3)}));
    }
  }
}

constexpr int K6_NSTG = 4;
constexpr int K6_STG = 2 * 9216 + 8 * 2 * 1024;
constexpr int K6_SMEM = K6_NSTG * K6_STG;
__global__ __launch_bounds__(512) void k_down(const u16* __restrict__ hh, const u16* __restrict__ wd,
                                              float* __restrict__ part) {
  extern __shared__ __attribute__((aligned(16))) char smem[];
  const int tid = threadIdx.x, lane = tid & 63, w = tid >> 6;
  const int xcd = blockIdx.x & 7, idx = blockIdx.x >> 3;
  const int sk = xcd >> 1, bn = idx & 3, bm = (xcd & 1) * 8 + (idx >> 2);
  float mu[2] = {0.f, 0.f}, rs[2] = {0.f, 0.f};
  f32x4 acc[2][9];
#pragma unroll
  for (int i = 0; i < 2; ++i)
#pragma unroll
    for (int j = 0; j < 9; ++j) acc[i][j] = (f32x4){0.f, 0.f, 0.f, 0.f};
  const u16* bp = wd + (size_t)sk * 1152 * D_ + bn * 144;
  gemm_core2<128, 1, 2, 8, K6_NSTG, false, false, 2, true>(hh + ((size_t)bm * 8 * 144 + sk * 36) * 512, 144, bp, bp, D_, 18, smem,
                                                           nullptr, 0, mu, rs, acc);
  f32x4 res[1][9];
  pair_reduce<4, 2>(acc, res, smem, w, lane);
  float* pp = part + (size_t)sk * S_ * D_;
  const int r = bm * 128 + (w & 3) * 32 + (w >> 2) * 16 + (lane & 15);
#pragma unroll
  for (int t = 0; t < 9; ++t) {
    const int n = bn * 144 + t * 16 + 4 * (lane >> 4);
    ST_OUT((f32x4*)(pp + (size_t)r * D_ + n), res[0][t]);
  }
}

__global__ __launch_bounds__(256) void k_final(const float* __restrict__ part, const u16* __restrict__ x1,
                                               const float* __restrict__ mod, u16* __restrict__ out) {
  const int i = blockIdx.x * 256 + threadIdx.x;
  const int e = i * 8;
  const int n = e % D_;
  f32x4 a0 = *(const f32x4*)(part + e), a1 = *(const f32x4*)(part + e + 4);
#pragma unroll
  for (int s = 1; s < 4; ++s) {
    a0 += *(const f32x4*)(part + (size_t)s * S_ * D_ + e);
    a1 += *(const f32x4*)(part + (size_t)s * S_ * D_ + e + 4);
  }
  const int row = e / D_, c = n >> 3;
  const u32x4 xv = *(const u32x4*)(x1 + ((size_t)((row >> 4) * 18 + (c >> 2)) * 64 + (c & 3) * 16 + (row & 15)) * 8);
  const f32x4 g0 = *(const f32x4*)(mod + 5 * 576 + n), g1 = *(const f32x4*)(mod + 5 * 576 + n + 4);
  u32x4 r;
  r[0] = pack2(bf2f(xv[0] & 0xffffu) + rbf(g0[0] * rbf(a0[0])), bfhi2f(xv[0]) + rbf(g0[1] * rbf(a0[1])));
  r[1] = pack2(bf2f(xv[1] & 0xffffu) + rbf(g0[2] * rbf(a0[2])), bfhi2f(xv[1]) + rbf(g0[3] * rbf(a0[3])));
  r[2] = pack2(bf2f(xv[2] & 0xffffu) + rbf(g1[0] * rbf(a1[0])), bfhi2f(xv[2]) + rbf(g1[1] * rbf(a1[1])));
  r[3] = pack2(bf2f(xv[3] & 0xffffu) + rbf(g1[2] * rbf(a1[2])), bfhi2f(xv[3]) + rbf(g1[3] * rbf(a1[3])));
  *(u32x4*)(out + e) = r;
}

extern "C" void kernel_launch(void* const* d_in, const int* in_sizes, int n_in, void* d_out, int out_size,
                              void* d_ws, size_t ws_size, hipStream_t stream) {
  const u16* x = (const u16*)d_in[0];
  const u16* wqkv = (const u16*)d_in[1];
  const u16* wo = (const u16*)d_in[2];
  const u16* wg = (const u16*)d_in[4];
  const u16* wu = (const u16*)d_in[5];
  const u16* wd = (const u16*)d_in[6];
  const u16* c = (const u16*)d_in[7];
  const u16* wmod = (const u16*)d_in[8];
  char* ws = (char*)d_ws;
  float* mod = (float*)(ws + WS_MOD);
  float* st1 = (float*)(ws + WS_ST1);
  u16* Qm = (u16*)(ws + WS_QM);
  u16* Qt = (u16*)(ws + WS_QT);
  u16* Km = (u16*)(ws + WS_KM);
  u16* Kt = (u16*)(ws + WS_KT);
  u16* Vt = (u16*)(ws + WS_VT);
  u16* o = (u16*)(ws + WS_O);
  u16* x1 = (u16*)(ws + WS_X1);
  float* st2 = (float*)(ws + WS_ST2);
  u16* hh = (u16*)(ws + WS_HH);
  u16* x1s = (u16*)(ws + WS_QM);
  float* cv = (float*)(ws + WS_CV);
  float* part = (float*)(ws + WS_PART);

  (void)hipFuncSetAttribute((const void*)k_qkv, hipFuncAttributeMaxDynamicSharedMemorySize, K2_SMEM);
  (void)hipFuncSetAttribute((const void*)k_attn, hipFuncAttributeMaxDynamicSharedMemorySize, A_SMEM);
  (void)hipFuncSetAttribute((const void*)k_wo, hipFuncAttributeMaxDynamicSharedMemorySize, K4_SMEM);
  (void)hipFuncSetAttribute((const void*)k_gateup, hipFuncAttributeMaxDynamicSharedMemorySize, K5_SMEM);
  (void)hipFuncSetAttribute((const void*)k_down, hipFuncAttributeMaxDynamicSharedMemorySize, K6_SMEM);

  k_prep<<<108 + 64, 256, 0, stream>>>(c, wmod, x, mod, st1);
  k_qkv<<<16 * 12 + 64, 512, K2_SMEM, stream>>>(x, wqkv, mod, st1, Qm, Qt, Km, Kt, Vt, wg, wu, cv);
  k_attn<<<256, 1024, A_SMEM, stream>>>(Qm, Qt, Km, Kt, Vt, o, wo, wg, wu, wd);
  k_wo<<<32 * 4, 256, K4_SMEM, stream>>>(o, wo, x, mod, x1, x1s, st2);
  k_gateup<<<8 * 32, 512, K5_SMEM, stream>>>(x1s, wg, wu, hh, x, st2, cv);
  k_down<<<16 * 4 * 4, 512, K6_SMEM, stream>>>(hh, wd, part);
  k_final<<<S_ * D_ / 8 / 256, 256, 0, stream>>>(part, x1, mod, (u16*)d_out);
}
```

```cpp
#include <hip/hip_runtime.h>
#include <hip/hip_bf16.h>
#include <stdint.h>

typedef short bf16x8 __attribute__((ext_vector_type(8)));
typedef short s16x4 __attribute__((ext_vector_type(4)));
typedef float f32x4 __attribute__((ext_vector_type(4)));
typedef float f32x16 __attribute__((ext_vector_type(16)));
typedef unsigned u32x4 __attribute__((ext_vector_type(4)));
typedef unsigned u32x2 __attribute__((ext_vector_type(2)));
typedef unsigned short u16;
typedef __bf16 bf2_t __attribute__((ext_vector_type(2)));
typedef float f2_t __attribute__((ext_vector_type(2)));

#define DI __device__ __forceinline__
#define ST_OUT(p, v) (*(p) = (v))
#define LDS_AS __attribute__((address_space(3)))

constexpr int S_ = 2048, D_ = 576, H_ = 32, DH_ = 18, F_ = 4608;
constexpr int NQKV = 3 * D_;

DI float bf2f(unsigned v) { return __uint_as_float(v << 16); }
DI float bfhi2f(unsigned v) { return __uint_as_float(v & 0xffff0000u); }
DI unsigned pack2(float lo, float hi) {
  bf2_t r = __builtin_convertvector((f2_t){lo, hi}, bf2_t);
  return __builtin_bit_cast(unsigned, r);
}
DI float rbf(float x) { return bf2f(pack2(x, 0.f) & 0xffffu); }

constexpr size_t WS_MOD = 0;
constexpr size_t WS_ST1 = 16384;
constexpr size_t WS_QM = 32768;
constexpr size_t WS_QT = WS_QM + (size_t)H_ * S_ * 16 * 2;
constexpr size_t WS_KM = WS_QT + (size_t)H_ * S_ * 2 * 2;
constexpr size_t WS_KT = WS_KM + (size_t)H_ * S_ * 16 * 2;
constexpr size_t WS_VT = WS_KT + (size_t)H_ * S_ * 2 * 2;
constexpr size_t WS_O = WS_VT + (size_t)H_ * DH_ * S_ * 2;
constexpr size_t WS_X1 = WS_O + (size_t)S_ * D_ * 2;
constexpr size_t WS_ST2 = WS_X1 + (size_t)S_ * D_ * 2;
constexpr size_t WS_HH = WS_ST2 + (size_t)S_ * 8 * 4;
constexpr size_t WS_PART = WS_HH + (size_t)S_ * F_ * 2;
constexpr size_t WS_WG = WS_PART + (size_t)4 * S_ * D_ * 4;
constexpr size_t WS_WU = WS_WG + (size_t)D_ * F_ * 2;
constexpr size_t WS_CV = WS_WU + (size_t)D_ * F_ * 2;
constexpr size_t WS_END = WS_CV + (size_t)4 * F_ * 4;

__global__ __launch_bounds__(256) void k_prep(const u16* __restrict__ c, const u16* __restrict__ wmod,
                                              const u16* __restrict__ x, float* __restrict__ mod,
                                              float* __restrict__ st1) {
  const int tid = threadIdx.x;
  if (blockIdx.x < 108) {
    __shared__ float red[64][33];
    const int tx = tid & 3, ty = tid >> 2;
    const int col0 = blockIdx.x * 32 + tx * 8;
    float acc[8];
#pragma unroll
    for (int j = 0; j < 8; ++j) acc[j] = 0.f;
    u32x4 w[9];
#pragma unroll
    for (int i = 0; i < 9; ++i) w[i] = *(const u32x4*)(wmod + (size_t)(ty + 64 * i) * 3456 + col0);
#pragma unroll
    for (int i = 0; i < 9; ++i) {
      const float cv = bf2f(c[ty + 64 * i]);
      const float s = rbf(cv / (1.f + __expf(-cv)));
#pragma unroll
      for (int j = 0; j < 4; ++j) {
        acc[2 * j] += s * bf2f(w[i][j] & 0xffffu);
        acc[2 * j + 1] += s * bfhi2f(w[i][j]);
      }
    }
#pragma unroll
    for (int j = 0; j < 8; ++j) red[ty][tx * 8 + j] = acc[j];
    __syncthreads();
    if (tid < 32) {
      float s = 0.f;
      for (int r = 0; r < 64; ++r) s += red[r][tid];
      mod[blockIdx.x * 32 + tid] = rbf(s);
    }
  } else {
    const int lane = tid & 63, w = tid >> 6;
    const int row = (blockIdx.x - 108) * 32 + w * 8 + (lane >> 3), j = lane & 7;
    const u16* xr = x + (size_t)row * D_;
    float v[72];
#pragma unroll
    for (int i = 0; i < 9; ++i) {
      u32x4 q = *(const u32x4*)(xr + (j + 8 * i) * 8);
#pragma unroll
      for (int e = 0; e < 4; ++e) {
        v[i * 8 + 2 * e] = bf2f(q[e] & 0xffffu);
        v[i * 8 + 2 * e + 1] = bfhi2f(q[e]);
      }
    }
    float s = 0.f;
#pragma unroll
    for (int i = 0; i < 72; ++i) s += v[i];
    s += __shfl_xor(s, 1); s += __shfl_xor(s, 2); s += __shfl_xor(s, 4);
    const float mean = s * (1.f / 576.f);
    float q2 = 0.f;
#pragma unroll
    for (int i = 0; i < 72; ++i) { const float d = v[i] - mean; q2 += d * d; }
    q2 += __shfl_xor(q2, 1); q2 += __shfl_xor(q2, 2); q2 += __shfl_xor(q2, 4);
    if (j == 0) {
      st1[row * 2] = mean;
      st1[row * 2 + 1] = 1.f / sqrtf(q2 * (1.f / 576.f) + 1e-5f);
    }
  }
}

template <int N> DI void wait_vm() { asm volatile("s_waitcnt vmcnt(%0)" ::"n"(N) : "memory"); }

#define ISSUE_DIV 2
template <int V> struct IC { static constexpr int value = V; };
struct NoMid { DI void operator()() const {} };
template <int BM, int NB, int NKS, int NW, int NSTG, bool LNA, bool SWAP, int MT = 4, bool AFRAG = false, class MID = NoMid>
DI void gemm_core2(const u16* __restrict__ Ag,
                   int lda, const u16* __restrict__ B0, const u16* __restrict__ B1,
                   int ldb, int nstages, char* smem, const float* lnw, int kbeg, const float (&mu)[MT],
                   const float (&rs)[MT], f32x4 (&acc)[MT][9], MID mid = MID()) {
  constexpr int RG = BM / (16 * MT);
  constexpr int NWORK = NB * NKS * RG / NW;
  constexpr int BBYTES = NB * NKS * 9216;
  constexpr int STG = BBYTES + (BM / 16) * NKS * 1024;
  constexpr int APW = (BM / 16) * NKS / NW;
  constexpr int BPW = NB * NKS * 576 / NW;
  constexpr int NBI = (BPW + 63) / 64;
  constexpr int BREM = BPW - (NBI - 1) * 64;
  constexpr int PER = APW + NBI;
  const int tid = threadIdx.x, lane = tid & 63;
  const int w = __builtin_amdgcn_readfirstlane(tid >> 6);
  const int rg = w % RG, wj = w / RG;
  const u16* Bw = ((BPW * w) / (576 * NKS) == 0) ? B0 : B1;
  unsigned bsrc[NBI];
#pragma unroll
  for (int i = 0; i < NBI; ++i) {
    const int c = BPW * w + 64 * i + (lane < BREM || i < NBI - 1 ? lane : 0);
    const int st = c / 576, cin = c - st * 576;
    const int ks = st % NKS;
    const int pos = cin / 18, cc = cin - pos * 18;
    const int r = (pos & 16) | ((pos & 1) << 3) | ((pos >> 1) & 7);
    bsrc[i] = (unsigned)((ks * 32 + r) * ldb + cc * 8);
  }
  unsigned asrc[APW];
#pragma unroll
  for (int i = 0; i < APW; ++i) {
    if constexpr (AFRAG) {
      const int blk = w * APW + i, ks = blk / (BM / 16), rt = blk - ks * (BM / 16);
      asrc[i] = (unsigned)(((rt * lda + ks) * 64 + lane) * 8);
    } else {
      const int blk = w * APW + i, kp = blk / (BM / 8), r8 = blk - kp * (BM / 8);
      asrc[i] = (unsigned)((r8 * 8 + (lane >> 3)) * lda + kp * 64 + (((lane & 7) ^ (lane >> 3)) * 8));
    }
  }
  auto issue_one = [&](int s, int i) {
    char* sb = smem + (s % NSTG) * STG;
    if (i < APW) {
      __builtin_amdgcn_global_load_lds((const void*)(Ag + s * NKS * (AFRAG ? 512 : 32) + asrc[i]), (LDS_AS void*)(sb + BBYTES + (w * APW + i) * 1024), 16, 0, 0);
    } else {
      const int j = i - APW;
      const u16* bb = Bw + (size_t)s * NKS * 32 * ldb;
      if (j < NBI - 1 || BREM == 64 || lane < BREM)
        __builtin_amdgcn_global_load_lds((const void*)(bb + bsrc[j]), (LDS_AS void*)(sb + (BPW * w + 64 * j) * 16), 16, 0, 0);
    }
  };
#pragma unroll
  for (int s = 0; s < NSTG - 1; ++s)
    if (s < nstages) {
#pragma unroll
      for (int i = 0; i < PER; ++i) issue_one(s, i);
    }
  mid();
  const int g = lane >> 4, q = (lane & 15) >> 2, p = lane & 3;
  const int brd = (16 * (g >> 1) + 2 * q + (g & 1)) * 288 + 8 * p;
  for (int s = 0; s < nstages; ++s) {
    if (s + NSTG - 2 < nstages) wait_vm<(NSTG - 2) * PER>();
    else wait_vm<0>();
    asm volatile("s_waitcnt lgkmcnt(0)" ::: "memory");
    __builtin_amdgcn_s_barrier();
    asm volatile("" ::: "memory");
    const bool more = (s + NSTG - 1 < nstages);
    char* sb = smem + (s % NSTG) * STG;
#pragma unroll
    for (int i = 0; i < NWORK; ++i) {
      const int u = wj * NWORK + i, b = u / NKS, ks = u - b * NKS;
      bf16x8 af[MT];
#pragma unroll
      for (int mt = 0; mt < MT; ++mt) {
        u32x4 raw;
        if constexpr (AFRAG) raw = *(const u32x4*)(sb + BBYTES + (ks * (BM / 16) + rg * MT + mt) * 1024 + lane * 16);
        else raw = *(const u32x4*)(sb + BBYTES + (ks >> 1) * BM * 128 + ((rg * MT + mt) * 16 + (lane & 15)) * 128 +
                                   (((4 * (ks & 1) + g) ^ (lane & 7)) * 16));
        if constexpr (LNA) {
          const int k = kbeg + (s * NKS + ks) * 32 + 8 * g;
          const f32x4 w0 = *(const f32x4*)(lnw + k), w1 = *(const f32x4*)(lnw + k + 4);
          const f32x4 b0 = *(const f32x4*)(lnw + 576 + k), b1 = *(const f32x4*)(lnw + 576 + k + 4);
          const float nm = -mu[mt] * rs[mt];
          u32x4 o;
#pragma unroll
          for (int e = 0; e < 4; ++e) {
            const float lo = fmaf(bf2f(raw[e] & 0xffffu), rs[mt], nm);
            const float hi = fmaf(bfhi2f(raw[e]), rs[mt], nm);
            const float wl = e < 2 ? w0[2 * e] : w1[2 * e - 4], wh = e < 2 ? w0[2 * e + 1] : w1[2 * e - 3];
            const float bl = e < 2 ? b0[2 * e] : b1[2 * e - 4], bh = e < 2 ? b0[2 * e + 1] : b1[2 * e - 3];
            o[e] = pack2(fmaf(lo, wl, bl), fmaf(hi, wh, bh));
          }
          raw = o;
        }
        af[mt] = __builtin_bit_cast(bf16x8, raw);
      }
      const char* bbase = sb + (b * NKS + ks) * 9216 + brd;
#pragma unroll
      for (int t = 0; t < 9; ++t) {
        s16x4 lo = __builtin_amdgcn_ds_read_tr16_b64_v4i16((LDS_AS s16x4*)(bbase + t * 32));
        s16x4 hi = __builtin_amdgcn_ds_read_tr16_b64_v4i16((LDS_AS s16x4*)(bbase + t * 32 + 2304));
        bf16x8 bf = __builtin_shufflevector(lo, hi, 0, 1, 2, 3, 4, 5, 6, 7);
#pragma unroll
        for (int mt = 0; mt < MT; ++mt) {
          if constexpr (SWAP) acc[mt][t] = __builtin_amdgcn_mfma_f32_16x16x32_bf16(af[mt], bf, acc[mt][t], 0, 0, 0);
          else acc[mt][t] = __builtin_amdgcn_mfma_f32_16x16x32_bf16(bf, af[mt], acc[mt][t], 0, 0, 0);
        }
        constexpr int NIT = (NWORK * 9 + 1) / ISSUE_DIV;
        const int it = i * 9 + t;
        if (more && it < NIT) {
#pragma unroll
          for (int e = it * PER / NIT; e < (it + 1) * PER / NIT; ++e) issue_one(s + NSTG - 1, e);
        }
      }
    }
  }
}

template <int X, int MT>
DI void pair_reduce(f32x4 (&acc)[MT][9], f32x4 (&res)[MT / 2][9], char* smem, int w, int lane) {
  constexpr int HM = MT / 2;
  f32x4* mine = (f32x4*)smem + w * HM * 9 * 64 + lane;
  f32x4* theirs = (f32x4*)smem + (w ^ X) * HM * 9 * 64 + lane;
  const bool hi = __builtin_amdgcn_readfirstlane(w & X) != 0;
  __syncthreads();
#pragma unroll
  for (int mt = 0; mt < HM; ++mt)
#pragma unroll
    for (int t = 0; t < 9; ++t) {
      f32x4 snd, kp;
#pragma unroll
      for (int j = 0; j < 4; ++j) {
        const float lo = acc[mt][t][j], up = acc[HM + mt][t][j];
        snd[j] = hi ? lo : up;
        kp[j] = hi ? up : lo;
      }
      mine[(mt * 9 + t) * 64] = snd;
      res[mt][t] = kp;
    }
  __syncthreads();
#pragma unroll
  for (int mt = 0; mt < HM; ++mt)
#pragma unroll
    for (int t = 0; t < 9; ++t) res[mt][t] += theirs[(mt * 9 + t) * 64];
}

constexpr int K2_NSTG = 4;
constexpr int K2_STG = 2 * 9216 + 128 * 128;
constexpr int K2_SMEM = K2_NSTG * K2_STG + 2 * 576 * 4;
__global__ __launch_bounds__(512) void k_qkv(const u16* __restrict__ x, const u16* __restrict__ wqkv,
                                             const float* __restrict__ mod, const float* __restrict__ st1,
                                             u16* __restrict__ Qm, u16* __restrict__ Qt, u16* __restrict__ Km,
                                             u16* __restrict__ Kt, u16* __restrict__ Vt, const u16* __restrict__ wg,
                                             const u16* __restrict__ wu, float* __restrict__ cv) {
  extern __shared__ __attribute__((aligned(16))) char smem[];
  const int tid = threadIdx.x, lane = tid & 63, w = tid >> 6;
  if (blockIdx.x >= 192) {
    const int j = blockIdx.x - 192, mtx = j >> 5, col0 = (j & 31) * 144;
    const u16* src = (mtx ? wu : wg) + col0;
    float* red = (float*)smem;
    if (tid < 432) {
      const int cc = tid % 18, rg = tid / 18;
      float c1[8], c2[8];
#pragma unroll
      for (int e = 0; e < 8; ++e) { c1[e] = 0.f; c2[e] = 0.f; }
      u32x4 v[24];
#pragma unroll
      for (int i = 0; i < 24; ++i) v[i] = *(const u32x4*)(src + (size_t)(rg + 24 * i) * F_ + cc * 8);
#pragma unroll
      for (int i = 0; i < 24; ++i) {
        const int k = rg + 24 * i;
        const float wk = 1.f + mod[4 * 576 + k], sk = mod[3 * 576 + k];
#pragma unroll
        for (int e = 0; e < 4; ++e) {
          const float lo = bf2f(v[i][e] & 0xffffu), hi = bfhi2f(v[i][e]);
          c1[2 * e] = fmaf(wk, lo, c1[2 * e]);
          c1[2 * e + 1] = fmaf(wk, hi, c1[2 * e + 1]);
          c2[2 * e] = fmaf(sk, lo, c2[2 * e]);
          c2[2 * e + 1] = fmaf(sk, hi, c2[2 * e + 1]);
        }
      }
#pragma unroll
      for (int e = 0; e < 8; ++e) {
        red[(rg * 144 + cc * 8 + e) * 2] = c1[e];
        red[(rg * 144 + cc * 8 + e) * 2 + 1] = c2[e];
      }
    }
    __syncthreads();
    if (tid < 288) {
      const int col = tid >> 1, which = tid & 1;
      float a = 0.f;
      for (int r = 0; r < 24; ++r) a += red[(r * 144 + col) * 2 + which];
      cv[(mtx * 2 + which) * F_ + col0 + col] = a;
    }
    return;
  }
  const int bm = blockIdx.x & 15, bn = blockIdx.x >> 4;
  float* lnw = (float*)(smem + K2_NSTG * K2_STG);
  const int row0 = bm * 128 + (w & 3) * 32;
  float mu[2], rs[2];
  auto mid = [&]() {
    for (int i = tid; i < 576; i += 512) {
      lnw[i] = 1.f + mod[576 + i];
      lnw[576 + i] = mod[i];
    }
#pragma unroll
    for (int mt = 0; mt < 2; ++mt) {
      const int r = row0 + mt * 16 + (lane & 15);
      mu[mt] = st1[r * 2];
      rs[mt] = st1[r * 2 + 1];
    }
    asm volatile("s_waitcnt vmcnt(0) lgkmcnt(0)" ::: "memory");
  };
  f32x4 acc[2][9];
#pragma unroll
  for (int i = 0; i < 2; ++i)
#pragma unroll
    for (int j = 0; j < 9; ++j) acc[i][j] = (f32x4){0.f, 0.f, 0.f, 0.f};
  mid(); __syncthreads();
  const int which = bn >> 2;
  const int mo = w >> 2;
  const int rw = row0 + mo * 16;
  const u16* bp = wqkv + bn * 144;
  f32x4 res[1][9];
  if (which == 2) {
    gemm_core2<128, 1, 2, 8, K2_NSTG, true, true, 2, false>(x + (size_t)bm * 128 * D_, D_, bp, bp, NQKV, 9, smem, lnw, 0, mu, rs, acc);
    pair_reduce<4, 2>(acc, res, smem, w, lane);
    const int g = lane >> 4;
#pragma unroll
    for (int t = 0; t < 9; ++t) {
      const int n = (bn & 3) * 144 + t * 16 + (lane & 15);
      const f32x4 v = res[0][t];
      const int pos = 16 * mo + 8 * (g & 1) + 4 * (g >> 1);
      *(u32x2*)(Vt + (size_t)n * S_ + row0 + pos) = (u32x2){pack2(v[0], v[1]), pack2(v[2], v[3])};
    }
    return;
  }
  gemm_core2<128, 1, 2, 8, K2_NSTG, true, false, 2, false>(x + (size_t)bm * 128 * D_, D_, bp, bp, NQKV, 9, smem, lnw, 0, mu, rs, acc);
  pair_reduce<4, 2>(acc, res, smem, w, lane);
  const float sc = which == 0 ? 0.23570226039551584f * 1.4426950408889634f : 1.f;
  char* tile = smem + 8 * 9 * 64 * 16;
#pragma unroll
  for (int t = 0; t < 9; ++t) {
    const f32x4 v = res[0][t];
    *(u32x2*)(tile + (rw - bm * 128 + (lane & 15)) * 296 + (t * 16 + 4 * (lane >> 4)) * 2) =
        (u32x2){pack2(v[0] * sc, v[1] * sc), pack2(v[2] * sc, v[3] * sc)};
  }
  __syncthreads();
  u16* dm = which == 0 ? Qm : Km;
  u16* dt = which == 0 ? Qt : Kt;
#pragma unroll
  for (int j = 0; j < 2; ++j) {
    const int i = tid + 512 * j, r = i & 127, hl = i >> 7;
    const unsigned* src = (const unsigned*)(tile + r * 296 + hl * 36);
    u32x4 a = {src[0], src[1], src[2], src[3]}, b = {src[4], src[5], src[6], src[7]};
    const unsigned tl = src[8];
    const size_t hr = (size_t)((bn & 3) * 8 + hl) * S_ + bm * 128 + r;
    *(u32x4*)(dm + hr * 16) = a;
    *(u32x4*)(dm + hr * 16 + 8) = b;
    *(unsigned*)(dt + hr * 2) = tl;
  }
}

constexpr int VROW = 4096 + 16;
constexpr int A_KM = 0, A_KT = 65536, A_VT = 65536 + 8192, A_END = A_VT + 20 * VROW;
constexpr int A_SMEM = A_END + 1024;
constexpr float ATT_THR = 8.f;
__global__ __launch_bounds__(1024) void k_attn(const u16* __restrict__ Qm, const u16* __restrict__ Qt,
                                               const u16* __restrict__ Km, const u16* __restrict__ Kt,
                                               const u16* __restrict__ Vt, u16* __restrict__ o,
                                               const u16* __restrict__ pf0, const u16* __restrict__ pf1,
                                               const u16* __restrict__ pf2, const u16* __restrict__ pf3) {
  extern __shared__ __attribute__((aligned(16))) char smem[];
  const int tid = threadIdx.x, lane = tid & 63;
  const int w = __builtin_amdgcn_readfirstlane(tid >> 6);
  const int bid = blockIdx.x;
  const int h = (bid & 7) * 4 + ((bid >> 3) & 3), qc = bid >> 5;
  const u16* kmg = Km + (size_t)h * S_ * 16;
  const u16* ktg = Kt + (size_t)h * S_ * 2;
  const u16* vtg = Vt + (size_t)h * DH_ * S_;
  const int r32 = lane & 31, hh = lane >> 5;
  const int qrow = qc * 256 + (w & 7) * 32 + r32;
  u32x4 qraw;
  unsigned qtw;
  {
    const u16* qp = Qm + ((size_t)h * S_ + qrow) * 16 + hh * 8;
    const u16* qtp = Qt + ((size_t)h * S_ + qrow) * 2;
    asm volatile("global_load_dwordx4 %0, %1, off" : "=&v"(qraw) : "v"(qp) : "memory");
    asm volatile("global_load_dword %0, %1, off" : "=&v"(qtw) : "v"(qtp) : "memory");
  }
#pragma unroll
  for (int c = 0; c < 4; ++c) {
    {
      const int hf = w >> 3, k0 = hf * 1024 + c * 256 + (w & 7) * 32;
      __builtin_amdgcn_global_load_lds((const void*)(kmg + k0 * 16 + lane * 8),
                                       (LDS_AS void*)(smem + A_KM + c * 16384 + w * 1024), 16, 0, 0);
    }
    {
      const int d = w;
      __builtin_amdgcn_global_load_lds((const void*)(vtg + d * S_ + hh * 1024 + c * 256 + r32 * 8),
                                       (LDS_AS void*)(smem + A_VT + d * VROW + c * 1024), 16, 0, 0);
    }
    if (w < 2) {
      const int d = 16 + w;
      __builtin_amdgcn_global_load_lds((const void*)(vtg + d * S_ + hh * 1024 + c * 256 + r32 * 8),
                                       (LDS_AS void*)(smem + A_VT + d * VROW + c * 1024), 16, 0, 0);
    } else if (w < 4) {
      const int hf = w - 2;
      __builtin_amdgcn_global_load_lds((const void*)(ktg + (hf * 1024 + c * 256) * 2 + lane * 8),
                                       (LDS_AS void*)(smem + A_KT + c * 2048 + hf * 1024), 16, 0, 0);
    }
  }
  if (tid < 512) {
    const int rr = 18 + (tid >> 8), cidx = tid & 255;
    const unsigned val = rr == 18 ? 0x3f803f80u : 0u;
    *(u32x4*)(smem + A_VT + rr * VROW + cidx * 16) = (u32x4){val, val, val, val};
  }
  const int khalf = w >> 3;

  const char* kmp = smem + A_KM + khalf * 8192 + r32 * 32 + hh * 16;
  const char* ktp = hh == 0 ? (smem + A_KT + khalf * 1024 + r32 * 4) : (smem + A_VT + 19 * VROW);
  const int ktc = hh == 0 ? 2048 : 0, ktt = hh == 0 ? 128 : 0;
  const int vrow = r32 < 19 ? r32 : 19;
  const char* vp = smem + A_VT + vrow * VROW + khalf * 512 + hh * 16;

  bf16x8 qf0, qf1;
  f32x16 oacc, negm;
#pragma unroll
  for (int i = 0; i < 16; ++i) { negm[i] = 0.f; oacc[i] = 0.f; }
  float m = 0.f;
  auto qk = [&](int tt) -> f32x16 {
    const int c = tt >> 3, t8 = tt & 7;
    bf16x8 kf0 = *(const bf16x8*)(kmp + c * 16384 + t8 * 1024);
    unsigned kt = *(const unsigned*)(ktp + c * ktc + t8 * ktt);
    bf16x8 kf1 = __builtin_bit_cast(bf16x8, (u32x4){kt, 0u, 0u, 0u});
    f32x16 s = __builtin_amdgcn_mfma_f32_32x32x16_bf16(kf0, qf0, negm, 0, 0, 0);
    return __builtin_amdgcn_mfma_f32_32x32x16_bf16(kf1, qf1, s, 0, 0, 0);
  };
  auto pv = [&](int tt, const f32x16& s) {
    const int c = tt >> 3, t8 = tt & 7;
    u32x4 p0, p1;
#pragma unroll
    for (int e = 0; e < 4; ++e) {
      p0[e] = pack2(__builtin_amdgcn_exp2f(s[2 * e]), __builtin_amdgcn_exp2f(s[2 * e + 1]));
      p1[e] = pack2(__builtin_amdgcn_exp2f(s[8 + 2 * e]), __builtin_amdgcn_exp2f(s[8 + 2 * e + 1]));
    }
    bf16x8 vf0 = *(const bf16x8*)(vp + c * 1024 + t8 * 64);
    bf16x8 vf1 = *(const bf16x8*)(vp + c * 1024 + t8 * 64 + 32);
    oacc = __builtin_amdgcn_mfma_f32_32x32x16_bf16(vf0, __builtin_bit_cast(bf16x8, p0), oacc, 0, 0, 0);
    oacc = __builtin_amdgcn_mfma_f32_32x32x16_bf16(vf1, __builtin_bit_cast(bf16x8, p1), oacc, 0, 0, 0);
  };
  auto fixmax = [&](f32x16& sc, f32x16& sn, bool first, bool has_next) {
    float tm = fmaxf(fmaxf(sc[0], sc[1]), sc[2]);
#pragma unroll
    for (int i = 3; i < 15; i += 2) tm = fmaxf(fmaxf(tm, sc[i]), sc[i + 1]);
    tm = fmaxf(tm, sc[15]);
    if (first || __any(tm > ATT_THR)) {
      float d = fmaxf(tm, __shfl_xor(tm, 32));
      if (!first) d = fmaxf(d, 0.f);
      m += d;
      const float al = __builtin_amdgcn_exp2f(-d);
#pragma unroll
      for (int i = 0; i < 16; ++i) {
        negm[i] = -m;
        sc[i] -= d;
        if (has_next) sn[i] -= d;
        if (!first) oacc[i] *= al;
      }
    }
  };
  const int nper = (w < 4) ? 3 : 2;
  f32x16 sA, sB;
#pragma unroll
  for (int c = 0; c < 4; ++c) {
    if (c < 3) {
      if (nper == 3) {
        if (c == 0) wait_vm<9>(); else if (c == 1) wait_vm<6>(); else wait_vm<3>();
      } else {
        if (c == 0) wait_vm<6>(); else if (c == 1) wait_vm<4>(); else wait_vm<2>();
      }
    } else {
      if (bid * 16 + w < 1088) wait_vm<7>(); else wait_vm<4>();
    }
    asm volatile("s_waitcnt lgkmcnt(0)" ::: "memory");
    __builtin_amdgcn_s_barrier();
    asm volatile("" ::: "memory");
    if (c == 0) {
      asm volatile("" : "+v"(qraw), "+v"(qtw));
      qf0 = __builtin_bit_cast(bf16x8, qraw);
      qf1 = __builtin_bit_cast(bf16x8, (u32x4){hh == 0 ? qtw : 0u, 0u, 0u, 0u});
    }
    if (c == 2) {
    {
      const int gw = bid * 16 + w;
#pragma unroll
      for (int i = 0; i < 4; ++i) {
        const u16* src;
        if (i == 0) src = pf0 + ((size_t)(gw % 648) * 512 + lane * 8);
        else if (i == 1) src = pf1 + ((size_t)gw * 512 + lane * 8);
        else if (i == 2) src = pf2 + ((size_t)gw * 512 + lane * 8);
        else src = pf3 + ((size_t)gw * 512 + lane * 8);
        __builtin_amdgcn_global_load_lds((const void*)src, (LDS_AS void*)(smem + A_END), 16, 0, 0);
      }
      if (gw < 1088) {
#pragma unroll
        for (int i = 1; i < 4; ++i) {
          const u16* src = (i == 1 ? pf1 : (i == 2 ? pf2 : pf3)) + ((size_t)(4096 + gw) * 512 + lane * 8);
          __builtin_amdgcn_global_load_lds((const void*)src, (LDS_AS void*)(smem + A_END), 16, 0, 0);
        }
      }
    }
    }
    sA = qk(8 * c);
    if (c == 0) fixmax(sA, sB, true, false);
#pragma unroll
    for (int t = 8 * c; t < 8 * c + 8; t += 2) {
      sB = qk(t + 1);
      pv(t, sA);
      if (t + 2 < 8 * c + 8) sA = qk(t + 2);
      pv(t + 1, sB);
    }
  }
  {
    bool bad = false;
#pragma unroll
    for (int i = 0; i < 11; ++i) bad |= !(fabsf(oacc[i]) < 1.0e37f);
    if (__any(bad)) {
#pragma unroll
      for (int i = 0; i < 16; ++i) { negm[i] = 0.f; oacc[i] = 0.f; }
      m = 0.f;
      sA = qk(0);
#pragma unroll 1
      for (int t = 0; t < 32; t += 2) {
        sB = qk(t + 1);
        fixmax(sA, sB, t == 0, true);
        pv(t, sA);
        const bool nx = (t + 2 < 32);
        if (nx) sA = qk(t + 2);
        fixmax(sB, sA, false, nx);
        pv(t + 1, sB);
      }
    }
  }
  __syncthreads();
  float* cb = (float*)(smem + A_KM) + (w & 7) * 64 * 17;
  if (khalf == 1) {
#pragma unroll
    for (int i = 0; i < 11; ++i) cb[i * 64 + lane] = oacc[i];
    cb[11 * 64 + lane] = m;
  }
  __syncthreads();
  if (khalf == 1) return;
  {
    const float m2 = cb[11 * 64 + lane];
    const float mm = fmaxf(m, m2);
    const float a1 = __builtin_amdgcn_exp2f(m - mm), a2 = __builtin_amdgcn_exp2f(m2 - mm);
#pragma unroll
    for (int i = 0; i < 11; ++i) oacc[i] = oacc[i] * a1 + cb[i * 64 + lane] * a2;
  }
  const float l = __shfl(oacc[10], r32);
  const float inv = 1.f / l;
  u16* op = o + (size_t)qrow * D_ + h * DH_;
  {
    u32x2 a = {pack2(oacc[0] * inv, oacc[1] * inv), pack2(oacc[2] * inv, oacc[3] * inv)};
    u32x2 b = {pack2(oacc[4] * inv, oacc[5] * inv), pack2(oacc[6] * inv, oacc[7] * inv)};
    unsigned* p32 = (unsigned*)(op + hh * 4);
    p32[0] = a[0]; p32[1] = a[1];
    p32[4] = b[0]; p32[5] = b[1];
    if (hh == 0) *(unsigned*)(op + 16) = pack2(oacc[8] * inv, oacc[9] * inv);
  }
}

constexpr int K4_NSTG = 5;
constexpr int K4_STG = 2 * 9216 + 64 * 128;
constexpr int K4_SMEM = K4_NSTG * K4_STG;
__global__ __launch_bounds__(256) void k_wo(const u16* __restrict__ o, const u16* __restrict__ wo,
                                            const u16* __restrict__ x, const float* __restrict__ mod,
                                            u16* __restrict__ x1, u16* __restrict__ x1s, float* __restrict__ st2) {
  extern __shared__ __attribute__((aligned(16))) char smem[];
  const int tid = threadIdx.x, lane = tid & 63, w = tid >> 6;
  const int bm = blockIdx.x & 31, bn = blockIdx.x >> 5;
  float mu[2] = {0.f, 0.f}, rs[2] = {0.f, 0.f};
  f32x4 acc[2][9];
#pragma unroll
  for (int i = 0; i < 2; ++i)
#pragma unroll
    for (int j = 0; j < 9; ++j) acc[i][j] = (f32x4){0.f, 0.f, 0.f, 0.f};
  const u16* bp = wo + bn * 144;
  const int r = bm * 64 + (w & 1) * 32 + (w >> 1) * 16 + (lane & 15);
  const float piv = bf2f(x[(size_t)r * D_]);
  u32x2 xv[9];
  f32x4 gv[9], cbv[9];
#pragma unroll
  for (int t = 0; t < 9; ++t) {
    const int n = bn * 144 + t * 16 + 4 * (lane >> 4);
    xv[t] = *(const u32x2*)(x + (size_t)r * D_ + n);
    gv[t] = *(const f32x4*)(mod + 2 * 576 + n);
    cbv[t] = *(const f32x4*)(mod + 4 * 576 + n);
  }
  asm volatile("" ::: "memory");
  gemm_core2<64, 1, 2, 4, K4_NSTG, false, false, 2>(o + (size_t)bm * 64 * D_, D_, bp, bp, D_, 9, smem, nullptr, 0, mu, rs, acc);
  f32x4 res[1][9];
  pair_reduce<2, 2>(acc, res, smem, w, lane);
  float s1 = 0.f, s2 = 0.f;
#pragma unroll
  for (int t = 0; t < 9; ++t) {
    const f32x4 v = res[0][t];
    const int n = bn * 144 + t * 16 + 4 * (lane >> 4);
    float y[4];
    y[0] = rbf(bf2f(xv[t][0] & 0xffffu) + rbf(gv[t][0] * rbf(v[0])));
    y[1] = rbf(bfhi2f(xv[t][0]) + rbf(gv[t][1] * rbf(v[1])));
    y[2] = rbf(bf2f(xv[t][1] & 0xffffu) + rbf(gv[t][2] * rbf(v[2])));
    y[3] = rbf(bfhi2f(xv[t][1]) + rbf(gv[t][3] * rbf(v[3])));
#pragma unroll
    for (int j = 0; j < 4; ++j) { const float d = y[j] - piv; s1 += d; s2 += d * d; }
    const size_t fo = ((size_t)((r >> 4) * 18 + (n >> 5)) * 64 + ((n >> 3) & 3) * 16 + (r & 15)) * 8 + (n & 7);
    *(u32x2*)(x1 + fo) = (u32x2){pack2(y[0], y[1]), pack2(y[2], y[3])};
    *(u32x2*)(x1s + fo) = (u32x2){pack2(y[0] * (1.f + cbv[t][0]), y[1] * (1.f + cbv[t][1])),
                                  pack2(y[2] * (1.f + cbv[t][2]), y[3] * (1.f + cbv[t][3]))};
  }
  s1 += __shfl_xor(s1, 16); s1 += __shfl_xor(s1, 32);
  s2 += __shfl_xor(s2, 16); s2 += __shfl_xor(s2, 32);
  if (lane < 16) *(f2_t*)(st2 + (size_t)r * 8 + bn * 2) = (f2_t){s1, s2};
}

constexpr int K5_NSTG = 4;
constexpr int K5_STG = 2 * 9216 + 16 * 1024;
constexpr int K5_SMEM = K5_NSTG * K5_STG + 576 * 4;
__global__ __launch_bounds__(512) void k_gateup(const u16* __restrict__ h2, const u16* __restrict__ wg,
                                                const u16* __restrict__ wu, u16* __restrict__ hh,
                                                const u16* __restrict__ xin, const float* __restrict__ st2,
                                                const float* __restrict__ cv) {
  extern __shared__ __attribute__((aligned(16))) char smem[];
  const int tid = threadIdx.x, lane = tid & 63, w = tid >> 6;
  const int bn = (blockIdx.x & 7) * 4 + ((blockIdx.x >> 3) & 3), bm = blockIdx.x >> 5;
  const int row0 = bm * 256 + (w & 3) * 64;
  float* cvl = (float*)(smem + K5_NSTG * K5_STG);
  float mu[4], rs[4];
  auto mid = [&]() {
    for (int i = tid; i < 576; i += 512) cvl[i] = cv[(i / 144) * F_ + bn * 144 + (i % 144)];
#pragma unroll
    for (int mt = 0; mt < 4; ++mt) {
      const int r = row0 + mt * 16 + (lane & 15);
      const f32x4 a = *(const f32x4*)(st2 + (size_t)r * 8), b = *(const f32x4*)(st2 + (size_t)r * 8 + 4);
      const float s1 = (a[0] + a[2]) + (b[0] + b[2]), s2 = (a[1] + a[3]) + (b[1] + b[3]);
      const float dm = s1 * (1.f / 576.f);
      const float var = fmaxf(s2 * (1.f / 576.f) - dm * dm, 0.f);
      rs[mt] = 1.f / sqrtf(var + 1e-5f);
      mu[mt] = -(bf2f(xin[(size_t)r * D_]) + dm) * rs[mt];
    }
    asm volatile("s_waitcnt vmcnt(0) lgkmcnt(0)" ::: "memory");
  };
  f32x4 acc[4][9];
#pragma unroll
  for (int i = 0; i < 4; ++i)
#pragma unroll
    for (int j = 0; j < 9; ++j) acc[i][j] = (f32x4){0.f, 0.f, 0.f, 0.f};
  gemm_core2<256, 2, 1, 8, K5_NSTG, false, false, 4, true>(h2 + (size_t)bm * 16 * 18 * 512, 18, wg + bn * 144, wu + bn * 144, F_,
                                                           18, smem, nullptr, 0, mu, rs, acc, mid);
  const float* c1p = cvl + (w >= 4 ? 288 : 0) + 4 * (lane >> 4);
  auto aff = [&](int mt, int t) -> f32x4 {
    const f32x4 c1 = *(const f32x4*)(c1p + t * 16), c2 = *(const f32x4*)(c1p + 144 + t * 16);
    f32x4 r;
#pragma unroll
    for (int j = 0; j < 4; ++j) r[j] = fmaf(acc[mt][t][j], rs[mt], fmaf(mu[mt], c1[j], c2[j]));
    return r;
  };
  __syncthreads();
  unsigned* exw = (unsigned*)smem + w * 36 * 64 + lane;
  const unsigned* exr = (const unsigned*)smem + (w ^ 4) * 36 * 64 + lane;
  const bool isu = __builtin_amdgcn_readfirstlane(w) >= 4;
  auto send = [&](auto OFF) {
#pragma unroll
    for (int mt = 0; mt < 2; ++mt)
#pragma unroll
      for (int t = 0; t < 9; ++t) {
        const f32x4 v = aff(OFF.value + mt, t);
        exw[((mt * 9 + t) * 2) * 64] = pack2(v[0], v[1]);
        exw[((mt * 9 + t) * 2 + 1) * 64] = pack2(v[2], v[3]);
      }
  };
  auto fin = [&](auto OFF) {
#pragma unroll
    for (int mt = 0; mt < 2; ++mt) {
      const int r = row0 + OFF.value * 16 + mt * 16 + (lane & 15);
#pragma unroll
      for (int t = 0; t < 9; ++t) {
        const int n = bn * 144 + t * 16 + 4 * (lane >> 4);
        const unsigned u0 = exr[((mt * 9 + t) * 2) * 64], u1 = exr[((mt * 9 + t) * 2 + 1) * 64];
        const f32x4 v = aff(OFF.value + mt, t);
        const float y0 = rbf(v[0]) * bf2f(u0 & 0xffffu), y1 = rbf(v[1]) * bfhi2f(u0);
        const float y2 = rbf(v[2]) * bf2f(u1 & 0xffffu), y3 = rbf(v[3]) * bfhi2f(u1);
        ST_OUT((u32x2*)(hh + ((size_t)((r >> 4) * 144 + (n >> 5)) * 64 + ((n >> 3) & 3) * 16 + (r & 15)) * 8 + (n & 7)),
               ((u32x2){pack2(y0, y1), pack2(y2, y3)}));
      }
    }
  };
  if (isu) send(IC<0>{}); else send(IC<2>{});
  __syncthreads();
  if (isu) fin(IC<2>{}); else fin(IC<0>{});
}

constexpr int K6_NSTG = 4;
constexpr int K6_STG = 2 * 9216 + 8 * 2 * 1024;
constexpr int K6_SMEM = K6_NSTG * K6_STG;
__global__ __launch_bounds__(512) void k_down(const u16* __restrict__ hh, const u16* __restrict__ wd,
                                              float* __restrict__ part) {
  extern __shared__ __attribute__((aligned(16))) char smem[];
  const int tid = threadIdx.x, lane = tid & 63, w = tid >> 6;
  const int xcd = blockIdx.x & 7, idx = blockIdx.x >> 3;
  const int sk = xcd >> 1, bn = idx & 3, bm = (xcd & 1) * 8 + (idx >> 2);
  float mu[2] = {0.f, 0.f}, rs[2] = {0.f, 0.f};
  f32x4 acc[2][9];
#pragma unroll
  for (int i = 0; i < 2; ++i)
#pragma unroll
    for (int j = 0; j < 9; ++j) acc[i][j] = (f32x4){0.f, 0.f, 0.f, 0.f};
  const u16* bp = wd + (size_t)sk * 1152 * D_ + bn * 144;
  gemm_core2<128, 1, 2, 8, K6_NSTG, false, false, 2, true>(hh + ((size_t)bm * 8 * 144 + sk * 36) * 512, 144, bp, bp, D_, 18, smem,
                                                           nullptr, 0, mu, rs, acc);
  f32x4 res[1][9];
  pair_reduce<4, 2>(acc, res, smem, w, lane);
  float* pp = part + (size_t)sk * S_ * D_;
  const int r = bm * 128 + (w & 3) * 32 + (w >> 2) * 16 + (lane & 15);
#pragma unroll
  for (int t = 0; t < 9; ++t) {
    const int n = bn * 144 + t * 16 + 4 * (lane >> 4);
    ST_OUT((f32x4*)(pp + (size_t)r * D_ + n), res[0][t]);
  }
}

__global__ __launch_bounds__(256) void k_final(const float* __restrict__ part, const u16* __restrict__ x1,
                                               const float* __restrict__ mod, u16* __restrict__ out) {
  const int i = blockIdx.x * 256 + threadIdx.x;
  const int e = i * 8;
  const int n = e % D_;
  f32x4 a0 = *(const f32x4*)(part + e), a1 = *(const f32x4*)(part + e + 4);
#pragma unroll
  for (int s = 1; s < 4; ++s) {
    a0 += *(const f32x4*)(part + (size_t)s * S_ * D_ + e);
    a1 += *(const f32x4*)(part + (size_t)s * S_ * D_ + e + 4);
  }
  const int row = e / D_, c = n >> 3;
  const u32x4 xv = *(const u32x4*)(x1 + ((size_t)((row >> 4) * 18 + (c >> 2)) * 64 + (c & 3) * 16 + (row & 15)) * 8);
  const f32x4 g0 = *(const f32x4*)(mod + 5 * 576 + n), g1 = *(const f32x4*)(mod + 5 * 576 + n + 4);
  u32x4 r;
  r[0] = pack2(bf2f(xv[0] & 0xffffu) + rbf(g0[0] * rbf(a0[0])), bfhi2f(xv[0]) + rbf(g0[1] * rbf(a0[1])));
  r[1] = pack2(bf2f(xv[1] & 0xffffu) + rbf(g0[2] * rbf(a0[2])), bfhi2f(xv[1]) + rbf(g0[3] * rbf(a0[3])));
  r[2] = pack2(bf2f(xv[2] & 0xffffu) + rbf(g1[0] * rbf(a1[0])), bfhi2f(xv[2]) + rbf(g1[1] * rbf(a1[1])));
  r[3] = pack2(bf2f(xv[3] & 0xffffu) + rbf(g1[2] * rbf(a1[2])), bfhi2f(xv[3]) + rbf(g1[3] * rbf(a1[3])));
  *(u32x4*)(out + e) = r;
}

extern "C" void kernel_launch(void* const* d_in, const int* in_sizes, int n_in, void* d_out, int out_size,
                              void* d_ws, size_t ws_size, hipStream_t stream) {
  const u16* x = (const u16*)d_in[0];
  const u16* wqkv = (const u16*)d_in[1];
  const u16* wo = (const u16*)d_in[2];
  const u16* wg = (const u16*)d_in[4];
  const u16* wu = (const u16*)d_in[5];
  const u16* wd = (const u16*)d_in[6];
  const u16* c = (const u16*)d_in[7];
  const u16* wmod = (const u16*)d_in[8];
  char* ws = (char*)d_ws;
  float* mod = (float*)(ws + WS_MOD);
  float* st1 = (float*)(ws + WS_ST1);
  u16* Qm = (u16*)(ws + WS_QM);
  u16* Qt = (u16*)(ws + WS_QT);
  u16* Km = (u16*)(ws + WS_KM);
  u16* Kt = (u16*)(ws + WS_KT);
  u16* Vt = (u16*)(ws + WS_VT);
  u16* o = (u16*)(ws + WS_O);
  u16* x1 = (u16*)(ws + WS_X1);
  float* st2 = (float*)(ws + WS_ST2);
  u16* hh = (u16*)(ws + WS_HH);
  u16* x1s = (u16*)(ws + WS_QM);
  float* cv = (float*)(ws + WS_CV);
  float* part = (float*)(ws + WS_PART);

  (void)hipFuncSetAttribute((const void*)k_qkv, hipFuncAttributeMaxDynamicSharedMemorySize, K2_SMEM);
  (void)hipFuncSetAttribute((const void*)k_attn, hipFuncAttributeMaxDynamicSharedMemorySize, A_SMEM);
  (void)hipFuncSetAttribute((const void*)k_wo, hipFuncAttributeMaxDynamicSharedMemorySize, K4_SMEM);
  (void)hipFuncSetAttribute((const void*)k_gateup, hipFuncAttributeMaxDynamicSharedMemorySize, K5_SMEM);
  (void)hipFuncSetAttribute((const void*)k_down, hipFuncAttributeMaxDynamicSharedMemorySize, K6_SMEM);

  k_prep<<<108 + 64, 256, 0, stream>>>(c, wmod, x, mod, st1);
  k_qkv<<<16 * 12 + 64, 512, K2_SMEM, stream>>>(x, wqkv, mod, st1, Qm, Qt, Km, Kt, Vt, wg, wu, cv);
  k_attn<<<256, 1024, A_SMEM, stream>>>(Qm, Qt, Km, Kt, Vt, o, wo, wg, wu, wd);
  k_wo<<<32 * 4, 256, K4_SMEM, stream>>>(o, wo, x, mod, x1, x1s, st2);
  k_gateup<<<8 * 32, 512, K5_SMEM, stream>>>(x1s, wg, wu, hh, x, st2, cv);
  k_down<<<16 * 4 * 4, 512, K6_SMEM, stream>>>(hh, wd, part);
  k_final<<<S_ * D_ / 8 / 256, 256, 0, stream>>>(part, x1, mod, (u16*)d_out);
}
```
